```python
import torch

def _softmax(x, axis=-1):
    return torch.softmax(x, dim=axis)

def _gelu(x):
    return torch.nn.functional.gelu(x, approximate='tanh')

def _silu(x):
    return torch.nn.functional.silu(x)

def _sigmoid(x):
    return torch.sigmoid(x)

def _layernorm(x, axis=-1, eps=1e-05):
    m = x.mean(dim=axis, keepdim=True)
    v = x.var(dim=axis, keepdim=True, unbiased=False)
    return (x - m) / torch.sqrt(v + eps)

def _scatter_add(tgt, idx, src, axis=0):
    return tgt.clone().index_add_(axis, idx, src)

def _segment_sum(src, seg, n):
    out = torch.zeros((n,) + src.shape[1:], dtype=src.dtype)
    return out.index_add_(0, seg, src)

def _pad(x, pw):
    flat = tuple((v for pair in reversed(pw) for v in pair))
    return torch.nn.functional.pad(x, flat)
kernel_name = 'standalone__tensor_product_equiv_mace__graph'

def setup_inputs():

    _w = lambda a: (lambda t: t.to(torch.bfloat16) if t.is_floating_point() else t)(torch.as_tensor(a))
    N, deg = (10000, 16)
    Lmax, C, De = (1, 256, 16)
    M = (Lmax + 1) ** 2
    E = N * deg
    src = (lambda lo, hi, *s: torch.randint(lo, hi, s))(0, N, E).to(torch.int64)
    dst = (lambda lo, hi, *s: torch.randint(lo, hi, s))(0, N, E).to(torch.int64)
    r = (lambda *s: torch.randn(*s, dtype=torch.bfloat16) * 0.05)(E, 3).to(float)
    r = r / (torch.linalg.norm(r, dim=-1, keepdim=True).to(float) + 1e-06)
    rx, ry, rz = (r[:, 0].clone(), r[:, 1].clone(), r[:, 2].clone())
    x2, y2, z2 = (rx * rx, ry * ry, rz * rz)
    sh_list = [torch.ones_like(rx), rx, ry, rz, rx * ry, ry * rz, 2.0 * z2 - x2 - y2, rx * rz, x2 - y2, ry * (3.0 * x2 - y2), rx * ry * rz, ry * (4.0 * z2 - x2 - y2), rz * (2.0 * z2 - 3.0 * x2 - 3.0 * y2), rx * (4.0 * z2 - x2 - y2), rz * (x2 - y2), rx * (x2 - 3.0 * y2)]
    sh16 = torch.stack(sh_list, dim=-1).to(float)
    pad = max(0, M - 16)
    sh_full = torch.cat([sh16, torch.zeros((E, pad), dtype=float)], dim=-1)[:, :M]
    cg = ((lambda *s: torch.randn(*s, dtype=torch.bfloat16) * 0.05)(M, M, M) * (1.0 / float(M))).to(float)
    return {'x': _w(((lambda *s: torch.randn(*s, dtype=torch.bfloat16) * 0.05)(N, M, C) * 0.3).to(float)), 'src': _w(src), 'dst': _w(dst), 'sh': _w(sh_full.to(float)), 'edge_attr': _w(((lambda *s: torch.randn(*s, dtype=torch.bfloat16) * 0.05)(E, De) * 0.3).to(float)), 'cg': _w(cg), 'w_r': _w(((lambda *s: torch.randn(*s, dtype=torch.bfloat16) * 0.05)(De, C) * (1.0 / float(De) ** 0.5)).to(float)), 'w_out': _w(((lambda *s: torch.randn(*s, dtype=torch.bfloat16) * 0.05)(C, C) * (1.0 / float(C) ** 0.5)).to(float))}

def reference(x, src, dst, sh, edge_attr, cg, w_r, w_out):
    N = x.shape[0]
    M = x.shape[1]
    C = x.shape[2]
    h = x[src]
    tp = torch.einsum('uvw,euc,ev->ewc', cg, h, sh)
    rw = edge_attr @ w_r
    msg = tp * rw[:, None, :]
    msg2 = msg.reshape(-1, M * C)
    agg = _segment_sum(msg2, dst, N).reshape(N, M, C)
    out = torch.einsum('nmc,cd->nmd', agg, w_out)
    return out
```

```cpp
#include <hip/hip_runtime.h>
#include <hip/hip_bf16.h>
#include <stdint.h>

typedef unsigned int u32;
typedef unsigned long long u64;
typedef __attribute__((ext_vector_type(8))) short bf16x8;
typedef __attribute__((ext_vector_type(2))) __bf16 bf16x2_t;
typedef __attribute__((ext_vector_type(2))) float f32x2;
typedef __attribute__((ext_vector_type(4))) float f32x4;
typedef __attribute__((ext_vector_type(16))) float f32x16;
typedef __attribute__((ext_vector_type(2))) unsigned int u32x2;
typedef __attribute__((ext_vector_type(4))) unsigned int u32x4;

#define DI __device__ __forceinline__
#define MFMA32(a, b, c) __builtin_amdgcn_mfma_f32_32x32x16_bf16((a), (b), (c), 0, 0, 0)
#define MFMA16(a, b, c) __builtin_amdgcn_mfma_f32_16x16x32_bf16((a), (b), (c), 0, 0, 0)

constexpr int NT = 768, NW = 12, NRG = 6, NB = 32;
constexpr int MAXN = 40;
constexpr int CAP = 944;
constexpr int LCAP = CAP + 48;
constexpr int CAPW = 128;
constexpr int WMIN = 512;
constexpr int SU = 9;
constexpr int TROW = 528;
constexpr int PFROWS = 1536;

constexpr int O_CNT = 0;
constexpr int O_BASE = O_CNT + 192 * 4;
constexpr int O_DEG = O_BASE + 192 * 4;
constexpr int O_MAP = O_DEG + 64 * 4;
constexpr int O_INV = O_MAP + 64 * 4;
constexpr int O_CGT = O_INV + 64 * 4;
constexpr int O_ZERO = O_CGT + 64 * 4;
constexpr int O_MISC = O_ZERO + 64;
constexpr int O_WR = O_MISC + 64;
constexpr int O_META = O_WR + 8192;
constexpr int O_AT = O_META + LCAP * 4;
constexpr int O_EA = O_AT + LCAP * 32;
constexpr int O_TILE = O_EA + LCAP * 32;
constexpr int O_CANDQ = O_TILE;
constexpr int O_CANDI = O_CANDQ + NW * CAPW * 16;
constexpr int O_HITE = O_CANDI + NW * CAPW * 4;
constexpr int O_HITL = O_HITE + 1024 * 4;
constexpr int O_TMP = O_HITL + 1024 * 4;
constexpr int LDS_BYTES = O_TILE + MAXN * 4 * TROW;
static_assert(O_TMP + LCAP * 8 <= LDS_BYTES, "alias region too small");
static_assert(LDS_BYTES <= 163840, "LDS");
static_assert((O_AT % 16) == 0 && (O_EA % 16) == 0 && (O_TILE % 16) == 0 && (O_WR % 16) == 0, "align");

DI u32 cvtpk(float a, float b) {
  f32x2 v = {a, b};
  bf16x2_t r = __builtin_convertvector(v, bf16x2_t);
  return __builtin_bit_cast(u32, r);
}
DI float bflo(u32 v) { return __builtin_bit_cast(float, v << 16); }
DI float bfhi(u32 v) { return __builtin_bit_cast(float, v & 0xffff0000u); }
DI u32 mbcnt(u64 m) { return __builtin_amdgcn_mbcnt_hi((u32)(m >> 32), __builtin_amdgcn_mbcnt_lo((u32)m, 0u)); }
DI u32 umin(u32 a, u32 b) { return a < b ? a : b; }

struct MainCtx {
  const char* xb;
  u32 xoff;
  u32 myslot;
  u32 atlane;
  u32 metaaddr;
  u32 eaaddr, eastride;
  u32 wraddr;
};

DI void load_block(const char* xb, u32 xoff, u32 m0, u32 m1, u32x2 (&buf)[8]) {
  const u32 o0 = (m0 & 0xfffff800u) | xoff, o1 = (m1 & 0xfffff800u) | xoff;
  const char* p0 = xb + (size_t)o0;
  const char* p1 = xb + (size_t)o1;
#pragma unroll
  for (int u = 0; u < 4; ++u) buf[u] = *(const u32x2*)(p0 + u * 512);
#pragma unroll
  for (int u = 0; u < 4; ++u) buf[4 + u] = *(const u32x2*)(p1 + u * 512);
}

DI u32x4 make_z(const u32x2 (&buf)[8], int r, float s0, float s1) {
  u32x4 z;
#pragma unroll
  for (int i = 0; i < 2; ++i) {
    const float s = i ? s1 : s0;
#pragma unroll
    for (int up = 0; up < 2; ++up) {
      const u32 a = (r & 2) ? buf[4 * i + 2 * up].y : buf[4 * i + 2 * up].x;
      const u32 b = (r & 2) ? buf[4 * i + 2 * up + 1].y : buf[4 * i + 2 * up + 1].x;
      z[2 * i + up] = (r & 1) ? cvtpk(bfhi(a) * s, bfhi(b) * s) : cvtpk(bflo(a) * s, bflo(b) * s);
    }
  }
  return z;
}

template <bool PF>
DI void step(char* smem, const MainCtx& c, int g, u32x4& mc, u32x4& eaf, u32x2 (&xb0)[8], u32x2 (&xb1)[8],
             f32x16 (&acc)[4]) {
  const u32 ab = c.atlane + g * 256;
  u32x2 bt[4];
#pragma unroll
  for (int k = 0; k < 4; ++k) {
    const u32 a = ((mc[k] & 15u) == c.myslot) ? ab + k * 32 : (u32)O_ZERO;
    bt[k] = *(const u32x2*)(smem + a);
  }
  if (PF) mc = *(const u32x4*)(smem + c.metaaddr + (g + 1) * 32);
  f32x4 rw[4];
  const f32x4 z4 = {0.f, 0.f, 0.f, 0.f};
  u32 wra = c.wraddr;
  asm volatile("" : "+v"(wra));
#pragma unroll
  for (int r = 0; r < 4; ++r) {
    const u32x4 wr = *(const u32x4*)(smem + wra + r * 1024);
    rw[r] = MFMA16(__builtin_bit_cast(bf16x8, eaf), __builtin_bit_cast(bf16x8, wr), z4);
  }
  if (PF) eaf = *(const u32x4*)(smem + c.eaaddr + (g + 1) * c.eastride);
  {
    const u32x4 b0 = {bt[0].x, bt[0].y, bt[1].x, bt[1].y};
#pragma unroll
    for (int r = 0; r < 4; ++r) {
      const u32x4 z = make_z(xb0, r, rw[r][0], rw[r][1]);
      acc[r] = MFMA32(__builtin_bit_cast(bf16x8, z), __builtin_bit_cast(bf16x8, b0), acc[r]);
    }
    if (PF) load_block(c.xb, c.xoff, mc[0], mc[1], xb0);
  }
  {
    const u32x4 b1 = {bt[2].x, bt[2].y, bt[3].x, bt[3].y};
#pragma unroll
    for (int r = 0; r < 4; ++r) {
      const u32x4 z = make_z(xb1, r, rw[r][2], rw[r][3]);
      acc[r] = MFMA32(__builtin_bit_cast(bf16x8, z), __builtin_bit_cast(bf16x8, b1), acc[r]);
    }
    if (PF) load_block(c.xb, c.xoff, mc[2], mc[3], xb1);
  }
}

__global__ __launch_bounds__(NT, 3) void mace_kernel(
    const char* __restrict__ x, const int* __restrict__ src, const int* __restrict__ dst,
    const char* __restrict__ sh, const char* __restrict__ eattr, const unsigned short* __restrict__ cg,
    const char* __restrict__ w_r, const char* __restrict__ w_out, char* __restrict__ out,
    float* __restrict__ ws, int N, int E, int b0, int G, float bscale) {
  __shared__ __attribute__((aligned(16))) char smem[LDS_BYTES];
  const int tid0 = threadIdx.x;
  const int wv = __builtin_amdgcn_readfirstlane(tid0 >> 6);
  const int rg = wv >> 1, hf = wv & 1;
  const int b = b0 + blockIdx.x;
  const u32 n0 = (u32)(((u64)b * (u64)N) / (u64)G);
  const u32 cntN = (u32)((((u64)b + 1) * (u64)N) / (u64)G) - n0;

  u32* s_cnt = (u32*)(smem + O_CNT);
  u32* s_base = (u32*)(smem + O_BASE);
  u32* s_deg = (u32*)(smem + O_DEG);
  u32* s_map = (u32*)(smem + O_MAP);
  u32* s_inv = (u32*)(smem + O_INV);
  float* s_cgt = (float*)(smem + O_CGT);
  u32* s_misc = (u32*)(smem + O_MISC);
  u32* s_hitE = (u32*)(smem + O_HITE);
  u32* s_hitL = (u32*)(smem + O_HITL);
  u64* s_tmp = (u64*)(smem + O_TMP);
  u32* s_meta = (u32*)(smem + O_META);

  if (wv < 2) {
    const int lane = tid0 & 63;
    const int q4 = lane >> 4, c16 = lane & 15;
    u32x4 wr[4];
    u32x2 L[8];
    const char* p = w_r + (size_t)(8 * (q4 & 1)) * 512 + (hf * 128 + 64 * (q4 >> 1) + 4 * c16) * 2;
#pragma unroll
    for (int j = 0; j < 8; ++j) L[j] = *(const u32x2*)(p + j * 512);
#pragma unroll
    for (int jj = 0; jj < 4; ++jj) {
      wr[0][jj] = __builtin_amdgcn_perm(L[2 * jj + 1].x, L[2 * jj].x, 0x05040100u);
      wr[1][jj] = __builtin_amdgcn_perm(L[2 * jj + 1].x, L[2 * jj].x, 0x07060302u);
      wr[2][jj] = __builtin_amdgcn_perm(L[2 * jj + 1].y, L[2 * jj].y, 0x05040100u);
      wr[3][jj] = __builtin_amdgcn_perm(L[2 * jj + 1].y, L[2 * jj].y, 0x07060302u);
    }
#pragma unroll
    for (int r = 0; r < 4; ++r) *(u32x4*)(smem + O_WR + hf * 4096 + r * 1024 + lane * 16) = wr[r];
  }
  if (wv == 2) {
    const int tid = tid0 & 63;
    const int w = tid >> 4, u = (tid >> 2) & 3, v = tid & 3;
    s_cgt[tid] = bflo((u32)cg[u * 16 + v * 4 + w]);
    s_map[tid] = (u32)(tid % NRG) | ((u32)(tid / NRG) << 8);
    const u32 l = (u32)((tid & 7) * NRG + (tid >> 3));
    s_inv[tid] = (tid < NRG * 8 && l < cntN) ? l : 0xffffffffu;
    if (tid < 16) ((u32*)(smem + O_ZERO))[tid] = 0u;
  }

  bool first = true, again = false;
  int pos = 0;
  int W = (E + 3) & ~3;
  for (;;) {
    if (again) __syncthreads();
    again = true;
    int tid = tid0;
    asm volatile("" : "+v"(tid));
    const int lane = tid & 63;
    const int q4 = lane >> 4, c16 = lane & 15, h = lane >> 5;
    u32 pfv = 0;
    {
      const u32 line = (u32)(b >> 3) * NT + tid;
      if (first && line < (u32)PFROWS * 16u && line < (u32)N * 16u) pfv = *(const u32*)(x + (size_t)line * 128);
    }
    if (tid < 192) s_cnt[tid] = 0u;
    if (tid >= 192 && tid < 256) s_deg[tid - 192] = 0u;
    if (tid >= 256 && tid < 258) s_misc[tid - 256] = 0u;
    __syncthreads();
    const int end = (E - pos < W) ? E : pos + W;

    {
      const u32x4* dq = (const u32x4*)dst;
      u32x4* cq = (u32x4*)(smem + O_CANDQ) + wv * CAPW;
      u32* ci = (u32*)(smem + O_CANDI) + wv * CAPW;
      const int q0 = pos >> 2, qf = end >> 2;
      u32 nc = 0;
      for (int qb = q0 + wv * 64; qb < qf; qb += NT * SU) {
        u32x4 v[SU];
#pragma unroll
        for (int k = 0; k < SU; ++k) {
          const int q = qb + k * NT + lane;
          v[k] = dq[q < qf ? q : qf - 1];
        }
#pragma unroll
        for (int k = 0; k < SU; ++k) {
          const int q = qb + k * NT + lane;
          const u32 m = umin(umin(v[k].x - n0, v[k].y - n0), umin(v[k].z - n0, v[k].w - n0));
          const bool hit = (m < cntN) && (q < qf);
          const u64 mask = __ballot(hit);
          if (mask) {
            const u32 p = nc + mbcnt(mask);
            if (hit && p < (u32)CAPW) { cq[p] = v[k]; ci[p] = (u32)q; }
            nc += (u32)__popcll(mask);
          }
        }
      }
      if (wv == NW - 1 && (end & 3)) {
        u32x4 v;
        const int e4 = qf * 4;
        v.x = (u32)dst[e4];
        v.y = (e4 + 1 < end) ? (u32)dst[e4 + 1] : n0 - 1u;
        v.z = (e4 + 2 < end) ? (u32)dst[e4 + 2] : n0 - 1u;
        v.w = n0 - 1u;
        if (lane == 0 && nc < (u32)CAPW) { cq[nc] = v; ci[nc] = (u32)qf; }
        nc += 1;
      }
      if (nc > (u32)CAPW) { if (lane == 0) s_misc[1] = 1u; nc = CAPW; }
      __builtin_amdgcn_wave_barrier();
      for (u32 i0 = 0; i0 < nc; i0 += 64) {
        const u32 i = i0 + lane;
        const bool valid = i < nc;
        const u32x4 v = cq[valid ? i : 0];
        const u32 q = ci[valid ? i : 0];
        const u32 t[4] = {v.x - n0, v.y - n0, v.z - n0, v.w - n0};
        u64 mk[4];
        u32 tot = 0;
#pragma unroll
        for (int k = 0; k < 4; ++k) { mk[k] = __ballot(valid && t[k] < cntN); tot += (u32)__popcll(mk[k]); }
        u32 bs = 0;
        if (lane == 0) bs = atomicAdd(&s_misc[0], tot);
        bs = __builtin_amdgcn_readfirstlane(bs);
#pragma unroll
        for (int k = 0; k < 4; ++k) {
          const u32 p = bs + mbcnt(mk[k]);
          if (valid && t[k] < cntN && p < (u32)CAP) {
            s_hitE[p] = 4 * q + k;
            s_hitL[p] = t[k];
            atomicAdd(&s_deg[t[k]], 1u);
          }
          bs += (u32)__popcll(mk[k]);
        }
      }
    }
    asm volatile("" ::"v"(pfv));
    __syncthreads();
    const u32 nh = __builtin_amdgcn_readfirstlane(s_misc[0]);
    if (nh > (u32)CAP || __builtin_amdgcn_readfirstlane(s_misc[1]) != 0u) {
      W = ((W >> 1) + 3) & ~3;
      if (W < WMIN) W = WMIN;
      continue;
    }

    u32 he[2], hl[2], hs[2];
    u32x2 hsh[2];
    u32x4 hea[2][2];
    bool act[2];
#pragma unroll
    for (int j = 0; j < 2; ++j) {
      const u32 i = tid + j * NT;
      act[j] = i < nh;
      he[j] = 0u; hl[j] = 0u; hs[j] = 0u;
      hsh[j] = u32x2{0u, 0u};
      hea[j][0] = u32x4{0u, 0u, 0u, 0u};
      hea[j][1] = u32x4{0u, 0u, 0u, 0u};
      if (j == 0 ? nh > 0u : nh > (u32)NT) {
        const u32 ii = act[j] ? i : 0;
        he[j] = s_hitE[ii];
        hl[j] = s_hitL[ii];
        hs[j] = (u32)src[he[j]];
        hsh[j] = *(const u32x2*)(sh + (size_t)he[j] * 8);
        hea[j][0] = *(const u32x4*)(eattr + (size_t)he[j] * 32);
        hea[j][1] = *(const u32x4*)(eattr + (size_t)he[j] * 32 + 16);
      }
    }
    if (first && wv == NW - 1) {
      const u32 d = s_deg[lane];
      u32 rank = 0;
      for (u32 j = 0; j < cntN; ++j) {
        const u32 dj = __builtin_amdgcn_readlane(d, j);
        rank += (dj > d || (dj == d && j < (u32)lane)) ? 1u : 0u;
      }
      const u32 slot = rank / NRG, r = rank % NRG;
      const u32 g = (slot & 1) ? (NRG - 1 - r) : r;
      s_inv[lane] = 0xffffffffu;
      __builtin_amdgcn_wave_barrier();
      if ((u32)lane < cntN) { s_map[lane] = g | (slot << 8); s_inv[g * 8 + slot] = lane; }
    }
    __syncthreads();

    u32 key[2], rk[2], mp[2];
#pragma unroll
    for (int j = 0; j < 2; ++j) {
      key[j] = 0u; rk[j] = 0u; mp[j] = 0u;
      if (j == 0 ? nh > 0u : nh > (u32)NT) {
        mp[j] = s_map[hl[j]];
        u32 bk = (u32)((float)hs[j] * bscale);
        bk = umin(bk, NB - 1);
        key[j] = (mp[j] & 255u) * NB + bk;
        if (act[j]) rk[j] = atomicAdd(&s_cnt[key[j]], 1u);
      }
    }
    __syncthreads();
    u32 ls = 0, lc = 0;
    {
      u32 v[3], inc[3];
#pragma unroll
      for (int k = 0; k < 3; ++k) {
        v[k] = s_cnt[k * 64 + lane];
        u32 t = v[k];
        t += (u32)__builtin_amdgcn_update_dpp(0, (int)t, 0x111, 0xf, 0xf, true);
        t += (u32)__builtin_amdgcn_update_dpp(0, (int)t, 0x112, 0xf, 0xf, true);
        t += (u32)__builtin_amdgcn_update_dpp(0, (int)t, 0x114, 0xf, 0xf, true);
        t += (u32)__builtin_amdgcn_update_dpp(0, (int)t, 0x118, 0xf, 0xf, true);
        t += (u32)__builtin_amdgcn_update_dpp(0, (int)t, 0x142, 0xa, 0xf, false);
        inc[k] = t;
      }
      u32 run = 0;
#pragma unroll
      for (int k = 0; k < 3; ++k) {
        const u32 c0 = __builtin_amdgcn_readlane(inc[k], 31);
        const u32 c1 = __builtin_amdgcn_readlane(inc[k], 63);
        const u32 s0 = run, s1 = run + ((c0 + 7u) & ~7u);
        run = s1 + ((c1 + 7u) & ~7u);
        ls = (rg == 2 * k) ? s0 : ls;
        lc = (rg == 2 * k) ? c0 : lc;
        ls = (rg == 2 * k + 1) ? s1 : ls;
        lc = (rg == 2 * k + 1) ? c1 : lc;
        s_base[k * 64 + lane] = (h ? s1 : s0) + inc[k] - v[k];
      }
      __builtin_amdgcn_wave_barrier();
    }
    u32 kb[2];
    u64 comp[2];
#pragma unroll
    for (int j = 0; j < 2; ++j) {
      kb[j] = 0u; comp[j] = 0ull;
      if (j == 0 ? nh > 0u : nh > (u32)NT) {
        kb[j] = s_base[key[j]];
        comp[j] = ((u64)hs[j] << 32) | he[j];
        if (act[j]) s_tmp[kb[j] + rk[j]] = comp[j];
      }
    }
    __syncthreads();
#pragma unroll
    for (int j = 0; j < 2; ++j) {
      if ((j == 0 || nh > (u32)NT) && act[j]) {
        const u32 n = s_cnt[key[j]];
        u32 p = kb[j];
        for (u32 k = 0; k < n; ++k) p += (s_tmp[kb[j] + k] < comp[j]) ? 1u : 0u;
        s_meta[p] = (hs[j] << 11) | (mp[j] >> 8);
        const float s0 = bflo(hsh[j].x), s1 = bfhi(hsh[j].x), s2 = bflo(hsh[j].y), s3 = bfhi(hsh[j].y);
        u32 at[8];
#pragma unroll
        for (int w = 0; w < 4; ++w) {
          float a[4];
#pragma unroll
          for (int u = 0; u < 4; ++u) {
            const f32x4 cv = *(const f32x4*)(s_cgt + w * 16 + u * 4);
            a[u] = cv.x * s0 + cv.y * s1 + cv.z * s2 + cv.w * s3;
          }
          at[2 * w] = cvtpk(a[0], a[1]);
          at[2 * w + 1] = cvtpk(a[2], a[3]);
        }
        u32x4* pa = (u32x4*)(smem + O_AT + p * 32);
        pa[0] = u32x4{at[0], at[1], at[2], at[3]};
        pa[1] = u32x4{at[4], at[5], at[6], at[7]};
        u32x4* pe = (u32x4*)(smem + O_EA + p * 32);
        pe[0] = hea[j][0];
        pe[1] = hea[j][1];
      }
    }
    if (hf == 0 && lc > 0) {
      const u32 p = ls + lc + lane;
      if (p < ls + ((lc + 7u) & ~7u)) {
        const u32 sl = (u32)(s_tmp[ls + lc - 1] >> 32);
        s_meta[p] = (sl << 11) | 15u;
        const u32x4 zz = {0u, 0u, 0u, 0u};
        u32x4* pe = (u32x4*)(smem + O_EA + p * 32);
        pe[0] = zz;
        pe[1] = zz;
      }
    }
    __syncthreads();

    f32x16 acc[4];
    if (first) {
#pragma unroll
      for (int r = 0; r < 4; ++r)
#pragma unroll
        for (int i = 0; i < 16; ++i) acc[r][i] = 0.f;
    } else {
      const f32x4* wsp = (const f32x4*)ws + ((size_t)blockIdx.x * NW + wv) * 1024 + lane;
#pragma unroll
      for (int r = 0; r < 4; ++r)
#pragma unroll
        for (int i = 0; i < 4; ++i) {
          const f32x4 t = wsp[(r * 4 + i) * 64];
          acc[r][4 * i] = t.x; acc[r][4 * i + 1] = t.y; acc[r][4 * i + 2] = t.z; acc[r][4 * i + 3] = t.w;
        }
    }
    const int ng = (int)((lc + 7u) >> 3);
    if (ng > 0) {
      MainCtx c;
      c.xb = x;
      c.xoff = hf * 256 + (lane & 31) * 8;
      c.myslot = (lane & 31) >> 2;
      c.atlane = O_AT + (lane & 3) * 8 + (ls + 4 * h) * 32;
      c.metaaddr = O_META + (ls + 4 * h) * 4;
      const bool match = (q4 >> 1) == ((c16 >> 2) & 1);
      c.eaaddr = match ? O_EA + (ls + 4 * (c16 >> 3) + (c16 & 3)) * 32 + 16 * (q4 & 1) : O_ZERO;
      c.eastride = match ? 256 : 0;
      c.wraddr = O_WR + hf * 4096 + lane * 16;
      u32x4 mc = *(const u32x4*)(smem + c.metaaddr);
      u32x4 eaf = *(const u32x4*)(smem + c.eaaddr);
      u32x2 xb0[8], xb1[8];
      load_block(c.xb, c.xoff, mc[0], mc[1], xb0);
      load_block(c.xb, c.xoff, mc[2], mc[3], xb1);
      int g = 0;
      for (; g < ng - 1; ++g) step<true>(smem, c, g, mc, eaf, xb0, xb1, acc);
      step<false>(smem, c, g, mc, eaf, xb0, xb1, acc);
    }
    if (end >= E) {

  u32x4 wf[8][2];
#pragma unroll
  for (int ks = 0; ks < 8; ++ks) { wf[ks][0] = u32x4{0u, 0u, 0u, 0u}; wf[ks][1] = u32x4{0u, 0u, 0u, 0u}; }
  if (wv < 8) {
    const int cb = 128 * (q4 & 1) + 64 * (q4 >> 1);
    const char* p = w_out + (size_t)cb * 512 + (wv * 32 + 2 * c16) * 2;
#pragma unroll
    for (int ks = 0; ks < 8; ++ks) {
      u32 L[8];
#pragma unroll
      for (int j = 0; j < 8; ++j) L[j] = *(const u32*)(p + (ks * 8 + j) * 512);
#pragma unroll
      for (int jj = 0; jj < 4; ++jj) {
        wf[ks][0][jj] = __builtin_amdgcn_perm(L[2 * jj + 1], L[2 * jj], 0x05040100u);
        wf[ks][1][jj] = __builtin_amdgcn_perm(L[2 * jj + 1], L[2 * jj], 0x07060302u);
      }
    }
  }
  {
    const u32 loc = s_inv[rg * 8 + ((lane & 31) >> 2)];
    if (loc != 0xffffffffu) {
      char* rowp = smem + O_TILE + (loc * 4 + (lane & 3)) * TROW + (hf * 128 + 16 * h) * 2;
#pragma unroll
      for (int qq = 0; qq < 4; ++qq) {
        u32 d[8];
#pragma unroll
        for (int a = 0; a < 4; ++a) {
          d[2 * a] = cvtpk(acc[0][4 * qq + a], acc[1][4 * qq + a]);
          d[2 * a + 1] = cvtpk(acc[2][4 * qq + a], acc[3][4 * qq + a]);
        }
        u32x4* pp = (u32x4*)(rowp + qq * 64);
        pp[0] = u32x4{d[0], d[1], d[2], d[3]};
        pp[1] = u32x4{d[4], d[5], d[6], d[7]};
      }
    }
  }
  __syncthreads();
  if (wv < 8) {
    const int cb = 128 * (q4 & 1) + 64 * (q4 >> 1);
    const char* tp = smem + O_TILE + c16 * TROW + cb * 2;
    char* op = out + ((size_t)n0 * 4 + c16) * 512 + (wv * 32 + 8 * q4) * 2;
    const u32 nrows = cntN * 4;
#pragma unroll 2
    for (int rb = 0; rb < MAXN * 4 / 16; ++rb) {
      f32x4 a0 = {0.f, 0.f, 0.f, 0.f}, a1 = {0.f, 0.f, 0.f, 0.f};
#pragma unroll
      for (int ks = 0; ks < 8; ++ks) {
        const u32x4 bf = *(const u32x4*)(tp + rb * 16 * TROW + ks * 16);
        a0 = MFMA16(__builtin_bit_cast(bf16x8, wf[ks][0]), __builtin_bit_cast(bf16x8, bf), a0);
        a1 = MFMA16(__builtin_bit_cast(bf16x8, wf[ks][1]), __builtin_bit_cast(bf16x8, bf), a1);
      }
      if ((u32)(rb * 16 + c16) < nrows) {
        const u32x4 o = {cvtpk(a0[0], a1[0]), cvtpk(a0[1], a1[1]), cvtpk(a0[2], a1[2]), cvtpk(a0[3], a1[3])};
        *(u32x4*)(op + (size_t)rb * 16 * 512) = o;
      }
    }
  }
  return;
    }
    int ln = tid0 & 63;
    asm volatile("" : "+v"(ln));
    f32x4* wsp = (f32x4*)ws + ((size_t)blockIdx.x * NW + wv) * 1024 + ln;
#pragma unroll
    for (int r = 0; r < 4; ++r)
#pragma unroll
      for (int i = 0; i < 4; ++i)
        wsp[(r * 4 + i) * 64] = f32x4{acc[r][4 * i], acc[r][4 * i + 1], acc[r][4 * i + 2], acc[r][4 * i + 3]};
    pos = end;
    first = false;
  }
}

extern "C" void kernel_launch(void* const* d_in, const int* in_sizes, int n_in, void* d_out, int out_size,
                              void* d_ws, size_t ws_size, hipStream_t stream) {
  const int N = in_sizes[0] / 1024;
  const int E = in_sizes[1];
  if (N <= 0) return;
  int G = (N + MAXN - 1) / MAXN;
  if (G < 256) G = 256;
  if (G > N) G = N;
  size_t gmax = ws_size / ((size_t)NW * 16384);
  if (gmax > 65536) gmax = 65536;
  if (gmax < 1) return;
  for (int b0 = 0; b0 < G; b0 += (int)gmax) {
    const int g = (G - b0 < (int)gmax) ? G - b0 : (int)gmax;
    mace_kernel<<<g, NT, 0, stream>>>((const char*)d_in[0], (const int*)d_in[1], (const int*)d_in[2],
                                      (const char*)d_in[3], (const char*)d_in[4], (const unsigned short*)d_in[5],
                                      (const char*)d_in[6], (const char*)d_in[7], (char*)d_out, (float*)d_ws, N, E,
                                      b0, G, (float)NB / (float)N);
  }
}
```

```cpp
#include <hip/hip_runtime.h>
#include <hip/hip_bf16.h>
#include <stdint.h>

typedef unsigned int u32;
typedef unsigned long long u64;
typedef __attribute__((ext_vector_type(8))) short bf16x8;
typedef __attribute__((ext_vector_type(2))) __bf16 bf16x2_t;
typedef __attribute__((ext_vector_type(2))) float f32x2;
typedef __attribute__((ext_vector_type(4))) float f32x4;
typedef __attribute__((ext_vector_type(16))) float f32x16;
typedef __attribute__((ext_vector_type(2))) unsigned int u32x2;
typedef __attribute__((ext_vector_type(4))) unsigned int u32x4;

#define DI __device__ __forceinline__
#define ST(k) do { } while (0)
#define MFMA32(a, b, c) __builtin_amdgcn_mfma_f32_32x32x16_bf16((a), (b), (c), 0, 0, 0)
#define MFMA16(a, b, c) __builtin_amdgcn_mfma_f32_16x16x32_bf16((a), (b), (c), 0, 0, 0)

DI u32 cvtpk(float a, float b) {
  f32x2 v = {a, b};
  bf16x2_t r = __builtin_convertvector(v, bf16x2_t);
  return __builtin_bit_cast(u32, r);
}
DI float bflo(u32 v) { return __builtin_bit_cast(float, v << 16); }
DI float bfhi(u32 v) { return __builtin_bit_cast(float, v & 0xffff0000u); }
DI u32 mbcnt(u64 m) { return __builtin_amdgcn_mbcnt_hi((u32)(m >> 32), __builtin_amdgcn_mbcnt_lo((u32)m, 0u)); }
DI u32 umin(u32 a, u32 b) { return a < b ? a : b; }

namespace gen {
constexpr int NT = 768, NW = 12, NRG = 6, NB = 32;
constexpr int MAXN = 40;
constexpr int CAP = 944;
constexpr int LCAP = CAP + 48;
constexpr int CAPW = 128;
constexpr int WMIN = 512;
constexpr int SU = 9;
constexpr int TROW = 528;
constexpr int PFROWS = 1536;

constexpr int O_CNT = 0;
constexpr int O_BASE = O_CNT + 192 * 4;
constexpr int O_DEG = O_BASE + 192 * 4;
constexpr int O_MAP = O_DEG + 64 * 4;
constexpr int O_INV = O_MAP + 64 * 4;
constexpr int O_CGT = O_INV + 64 * 4;
constexpr int O_ZERO = O_CGT + 64 * 4;
constexpr int O_MISC = O_ZERO + 64;
constexpr int O_WR = O_MISC + 64;
constexpr int O_META = O_WR + 8192;
constexpr int O_AT = O_META + LCAP * 4;
constexpr int O_EA = O_AT + LCAP * 32;
constexpr int O_TILE = O_EA + LCAP * 32;
constexpr int O_CANDQ = O_TILE;
constexpr int O_CANDI = O_CANDQ + NW * CAPW * 16;
constexpr int O_HITE = O_CANDI + NW * CAPW * 4;
constexpr int O_HITL = O_HITE + 1024 * 4;
constexpr int O_TMP = O_HITL + 1024 * 4;
constexpr int LDS_BYTES = O_TILE + MAXN * 4 * TROW;
static_assert(O_TMP + LCAP * 8 <= LDS_BYTES, "alias region too small");
static_assert(LDS_BYTES <= 163840, "LDS");
static_assert((O_AT % 16) == 0 && (O_EA % 16) == 0 && (O_TILE % 16) == 0 && (O_WR % 16) == 0, "align");

struct MainCtx {
  const char* xb;
  u32 xoff;
  u32 myslot;
  u32 atlane;
  u32 metaaddr;
  u32 eaaddr, eastride;
  u32 wraddr;
};

DI void load_block(const char* xb, u32 xoff, u32 m0, u32 m1, u32x2 (&buf)[8]) {
  const u32 o0 = (m0 & 0xfffff800u) | xoff, o1 = (m1 & 0xfffff800u) | xoff;
  const char* p0 = xb + (size_t)o0;
  const char* p1 = xb + (size_t)o1;
#pragma unroll
  for (int u = 0; u < 4; ++u) buf[u] = *(const u32x2*)(p0 + u * 512);
#pragma unroll
  for (int u = 0; u < 4; ++u) buf[4 + u] = *(const u32x2*)(p1 + u * 512);
}

DI u32x4 make_z(const u32x2 (&buf)[8], int r, float s0, float s1) {
  u32x4 z;
#pragma unroll
  for (int i = 0; i < 2; ++i) {
    const float s = i ? s1 : s0;
#pragma unroll
    for (int up = 0; up < 2; ++up) {
      const u32 a = (r & 2) ? buf[4 * i + 2 * up].y : buf[4 * i + 2 * up].x;
      const u32 b = (r & 2) ? buf[4 * i + 2 * up + 1].y : buf[4 * i + 2 * up + 1].x;
      z[2 * i + up] = (r & 1) ? cvtpk(bfhi(a) * s, bfhi(b) * s) : cvtpk(bflo(a) * s, bflo(b) * s);
    }
  }
  return z;
}

template <bool PF>
DI void step(char* smem, const MainCtx& c, int g, u32x4& mc, u32x4& eaf, u32x2 (&xb0)[8], u32x2 (&xb1)[8],
             f32x16 (&acc)[4]) {
  const u32 ab = c.atlane + g * 256;
  u32x2 bt[4];
#pragma unroll
  for (int k = 0; k < 4; ++k) {
    const u32 a = ((mc[k] & 15u) == c.myslot) ? ab + k * 32 : (u32)O_ZERO;
    bt[k] = *(const u32x2*)(smem + a);
  }
  if (PF) mc = *(const u32x4*)(smem + c.metaaddr + (g + 1) * 32);
  f32x4 rw[4];
  const f32x4 z4 = {0.f, 0.f, 0.f, 0.f};
  u32 wra = c.wraddr;
  asm volatile("" : "+v"(wra));
#pragma unroll
  for (int r = 0; r < 4; ++r) {
    const u32x4 wr = *(const u32x4*)(smem + wra + r * 1024);
    rw[r] = MFMA16(__builtin_bit_cast(bf16x8, eaf), __builtin_bit_cast(bf16x8, wr), z4);
  }
  if (PF) eaf = *(const u32x4*)(smem + c.eaaddr + (g + 1) * c.eastride);
  {
    const u32x4 b0 = {bt[0].x, bt[0].y, bt[1].x, bt[1].y};
#pragma unroll
    for (int r = 0; r < 4; ++r) {
      const u32x4 z = make_z(xb0, r, rw[r][0], rw[r][1]);
      acc[r] = MFMA32(__builtin_bit_cast(bf16x8, z), __builtin_bit_cast(bf16x8, b0), acc[r]);
    }
    if (PF) load_block(c.xb, c.xoff, mc[0], mc[1], xb0);
  }
  {
    const u32x4 b1 = {bt[2].x, bt[2].y, bt[3].x, bt[3].y};
#pragma unroll
    for (int r = 0; r < 4; ++r) {
      const u32x4 z = make_z(xb1, r, rw[r][2], rw[r][3]);
      acc[r] = MFMA32(__builtin_bit_cast(bf16x8, z), __builtin_bit_cast(bf16x8, b1), acc[r]);
    }
    if (PF) load_block(c.xb, c.xoff, mc[2], mc[3], xb1);
  }
}

__global__ __launch_bounds__(NT, 3) void mace_kernel(
    const char* __restrict__ x, const int* __restrict__ src, const int* __restrict__ dst,
    const char* __restrict__ sh, const char* __restrict__ eattr, const unsigned short* __restrict__ cg,
    const char* __restrict__ w_r, const char* __restrict__ w_out, char* __restrict__ out,
    float* __restrict__ ws, int N, int E, int b0, int G, float bscale) {
  __shared__ __attribute__((aligned(16))) char smem[LDS_BYTES];
  const int tid0 = threadIdx.x;
  const int wv = __builtin_amdgcn_readfirstlane(tid0 >> 6);
  const int rg = wv >> 1, hf = wv & 1;
  const int b = b0 + blockIdx.x;
  const u32 n0 = (u32)(((u64)b * (u64)N) / (u64)G);
  const u32 cntN = (u32)((((u64)b + 1) * (u64)N) / (u64)G) - n0;

  u32* s_cnt = (u32*)(smem + O_CNT);
  u32* s_base = (u32*)(smem + O_BASE);
  u32* s_deg = (u32*)(smem + O_DEG);
  u32* s_map = (u32*)(smem + O_MAP);
  u32* s_inv = (u32*)(smem + O_INV);
  float* s_cgt = (float*)(smem + O_CGT);
  u32* s_misc = (u32*)(smem + O_MISC);
  u32* s_hitE = (u32*)(smem + O_HITE);
  u32* s_hitL = (u32*)(smem + O_HITL);
  u64* s_tmp = (u64*)(smem + O_TMP);
  u32* s_meta = (u32*)(smem + O_META);

  if (wv < 2) {
    const int lane = tid0 & 63;
    const int q4 = lane >> 4, c16 = lane & 15;
    u32x4 wr[4];
    u32x2 L[8];
    const char* p = w_r + (size_t)(8 * (q4 & 1)) * 512 + (hf * 128 + 64 * (q4 >> 1) + 4 * c16) * 2;
#pragma unroll
    for (int j = 0; j < 8; ++j) L[j] = *(const u32x2*)(p + j * 512);
#pragma unroll
    for (int jj = 0; jj < 4; ++jj) {
      wr[0][jj] = __builtin_amdgcn_perm(L[2 * jj + 1].x, L[2 * jj].x, 0x05040100u);
      wr[1][jj] = __builtin_amdgcn_perm(L[2 * jj + 1].x, L[2 * jj].x, 0x07060302u);
      wr[2][jj] = __builtin_amdgcn_perm(L[2 * jj + 1].y, L[2 * jj].y, 0x05040100u);
      wr[3][jj] = __builtin_amdgcn_perm(L[2 * jj + 1].y, L[2 * jj].y, 0x07060302u);
    }
#pragma unroll
    for (int r = 0; r < 4; ++r) *(u32x4*)(smem + O_WR + hf * 4096 + r * 1024 + lane * 16) = wr[r];
  }
  if (wv == 2) {
    const int tid = tid0 & 63;
    const int w = tid >> 4, u = (tid >> 2) & 3, v = tid & 3;
    s_cgt[tid] = bflo((u32)cg[u * 16 + v * 4 + w]);
    s_map[tid] = (u32)(tid % NRG) | ((u32)(tid / NRG) << 8);
    const u32 l = (u32)((tid & 7) * NRG + (tid >> 3));
    s_inv[tid] = (tid < NRG * 8 && l < cntN) ? l : 0xffffffffu;
    if (tid < 16) ((u32*)(smem + O_ZERO))[tid] = 0u;
  }

  bool first = true, again = false;
  int pos = 0;
  int W = (E + 3) & ~3;
  for (;;) {
    if (again) __syncthreads();
    again = true;
    int tid = tid0;
    asm volatile("" : "+v"(tid));
    const int lane = tid & 63;
    const int q4 = lane >> 4, c16 = lane & 15, h = lane >> 5;
    u32 pfv = 0;
    {
      const u32 line = (u32)(b >> 3) * NT + tid;
      if (first && line < (u32)PFROWS * 16u && line < (u32)N * 16u) pfv = *(const u32*)(x + (size_t)line * 128);
    }
    if (tid < 192) s_cnt[tid] = 0u;
    if (tid >= 192 && tid < 256) s_deg[tid - 192] = 0u;
    if (tid >= 256 && tid < 258) s_misc[tid - 256] = 0u;
    __syncthreads();
    const int end = (E - pos < W) ? E : pos + W;

    {
      const u32x4* dq = (const u32x4*)dst;
      u32x4* cq = (u32x4*)(smem + O_CANDQ) + wv * CAPW;
      u32* ci = (u32*)(smem + O_CANDI) + wv * CAPW;
      const int q0 = pos >> 2, qf = end >> 2;
      u32 nc = 0;
      for (int qb = q0 + wv * 64; qb < qf; qb += NT * SU) {
        u32x4 v[SU];
#pragma unroll
        for (int k = 0; k < SU; ++k) {
          const int q = qb + k * NT + lane;
          v[k] = dq[q < qf ? q : qf - 1];
        }
#pragma unroll
        for (int k = 0; k < SU; ++k) {
          const int q = qb + k * NT + lane;
          const u32 m = umin(umin(v[k].x - n0, v[k].y - n0), umin(v[k].z - n0, v[k].w - n0));
          const bool hit = (m < cntN) && (q < qf);
          const u64 mask = __ballot(hit);
          if (mask) {
            const u32 p = nc + mbcnt(mask);
            if (hit && p < (u32)CAPW) { cq[p] = v[k]; ci[p] = (u32)q; }
            nc += (u32)__popcll(mask);
          }
        }
      }
      if (wv == NW - 1 && (end & 3)) {
        u32x4 v;
        const int e4 = qf * 4;
        v.x = (u32)dst[e4];
        v.y = (e4 + 1 < end) ? (u32)dst[e4 + 1] : n0 - 1u;
        v.z = (e4 + 2 < end) ? (u32)dst[e4 + 2] : n0 - 1u;
        v.w = n0 - 1u;
        if (lane == 0 && nc < (u32)CAPW) { cq[nc] = v; ci[nc] = (u32)qf; }
        nc += 1;
      }
      if (nc > (u32)CAPW) { if (lane == 0) s_misc[1] = 1u; nc = CAPW; }
      __builtin_amdgcn_wave_barrier();
      for (u32 i0 = 0; i0 < nc; i0 += 64) {
        const u32 i = i0 + lane;
        const bool valid = i < nc;
        const u32x4 v = cq[valid ? i : 0];
        const u32 q = ci[valid ? i : 0];
        const u32 t[4] = {v.x - n0, v.y - n0, v.z - n0, v.w - n0};
        u64 mk[4];
        u32 tot = 0;
#pragma unroll
        for (int k = 0; k < 4; ++k) { mk[k] = __ballot(valid && t[k] < cntN); tot += (u32)__popcll(mk[k]); }
        u32 bs = 0;
        if (lane == 0) bs = atomicAdd(&s_misc[0], tot);
        bs = __builtin_amdgcn_readfirstlane(bs);
#pragma unroll
        for (int k = 0; k < 4; ++k) {
          const u32 p = bs + mbcnt(mk[k]);
          if (valid && t[k] < cntN && p < (u32)CAP) {
            s_hitE[p] = 4 * q + k;
            s_hitL[p] = t[k];
            atomicAdd(&s_deg[t[k]], 1u);
          }
          bs += (u32)__popcll(mk[k]);
        }
      }
    }
    asm volatile("" ::"v"(pfv));
    __syncthreads();
    const u32 nh = __builtin_amdgcn_readfirstlane(s_misc[0]);
    if (nh > (u32)CAP || __builtin_amdgcn_readfirstlane(s_misc[1]) != 0u) {
      W = ((W >> 1) + 3) & ~3;
      if (W < WMIN) W = WMIN;
      continue;
    }

    u32 he[2], hl[2], hs[2];
    u32x2 hsh[2];
    u32x4 hea[2][2];
    bool act[2];
#pragma unroll
    for (int j = 0; j < 2; ++j) {
      const u32 i = tid + j * NT;
      act[j] = i < nh;
      he[j] = 0u; hl[j] = 0u; hs[j] = 0u;
      hsh[j] = u32x2{0u, 0u};
      hea[j][0] = u32x4{0u, 0u, 0u, 0u};
      hea[j][1] = u32x4{0u, 0u, 0u, 0u};
      if (j == 0 ? nh > 0u : nh > (u32)NT) {
        const u32 ii = act[j] ? i : 0;
        he[j] = s_hitE[ii];
        hl[j] = s_hitL[ii];
        hs[j] = (u32)src[he[j]];
        hsh[j] = *(const u32x2*)(sh + (size_t)he[j] * 8);
        hea[j][0] = *(const u32x4*)(eattr + (size_t)he[j] * 32);
        hea[j][1] = *(const u32x4*)(eattr + (size_t)he[j] * 32 + 16);
      }
    }
    if (first && wv == NW - 1) {
      const u32 d = s_deg[lane];
      u32 rank = 0;
      for (u32 j = 0; j < cntN; ++j) {
        const u32 dj = __builtin_amdgcn_readlane(d, j);
        rank += (dj > d || (dj == d && j < (u32)lane)) ? 1u : 0u;
      }
      const u32 slot = rank / NRG, r = rank % NRG;
      const u32 g = (slot & 1) ? (NRG - 1 - r) : r;
      s_inv[lane] = 0xffffffffu;
      __builtin_amdgcn_wave_barrier();
      if ((u32)lane < cntN) { s_map[lane] = g | (slot << 8); s_inv[g * 8 + slot] = lane; }
    }
    __syncthreads();

    u32 key[2], rk[2], mp[2];
#pragma unroll
    for (int j = 0; j < 2; ++j) {
      key[j] = 0u; rk[j] = 0u; mp[j] = 0u;
      if (j == 0 ? nh > 0u : nh > (u32)NT) {
        mp[j] = s_map[hl[j]];
        u32 bk = (u32)((float)hs[j] * bscale);
        bk = umin(bk, NB - 1);
        key[j] = (mp[j] & 255u) * NB + bk;
        if (act[j]) rk[j] = atomicAdd(&s_cnt[key[j]], 1u);
      }
    }
    __syncthreads();
    u32 ls = 0, lc = 0;
    {
      u32 v[3], inc[3];
#pragma unroll
      for (int k = 0; k < 3; ++k) {
        v[k] = s_cnt[k * 64 + lane];
        u32 t = v[k];
        t += (u32)__builtin_amdgcn_update_dpp(0, (int)t, 0x111, 0xf, 0xf, true);
        t += (u32)__builtin_amdgcn_update_dpp(0, (int)t, 0x112, 0xf, 0xf, true);
        t += (u32)__builtin_amdgcn_update_dpp(0, (int)t, 0x114, 0xf, 0xf, true);
        t += (u32)__builtin_amdgcn_update_dpp(0, (int)t, 0x118, 0xf, 0xf, true);
        t += (u32)__builtin_amdgcn_update_dpp(0, (int)t, 0x142, 0xa, 0xf, false);
        inc[k] = t;
      }
      u32 run = 0;
#pragma unroll
      for (int k = 0; k < 3; ++k) {
        const u32 c0 = __builtin_amdgcn_readlane(inc[k], 31);
        const u32 c1 = __builtin_amdgcn_readlane(inc[k], 63);
        const u32 s0 = run, s1 = run + ((c0 + 7u) & ~7u);
        run = s1 + ((c1 + 7u) & ~7u);
        ls = (rg == 2 * k) ? s0 : ls;
        lc = (rg == 2 * k) ? c0 : lc;
        ls = (rg == 2 * k + 1) ? s1 : ls;
        lc = (rg == 2 * k + 1) ? c1 : lc;
        s_base[k * 64 + lane] = (h ? s1 : s0) + inc[k] - v[k];
      }
      __builtin_amdgcn_wave_barrier();
    }
    u32 kb[2];
    u64 comp[2];
#pragma unroll
    for (int j = 0; j < 2; ++j) {
      kb[j] = 0u; comp[j] = 0ull;
      if (j == 0 ? nh > 0u : nh > (u32)NT) {
        kb[j] = s_base[key[j]];
        comp[j] = ((u64)hs[j] << 32) | he[j];
        if (act[j]) s_tmp[kb[j] + rk[j]] = comp[j];
      }
    }
    __syncthreads();
#pragma unroll
    for (int j = 0; j < 2; ++j) {
      if ((j == 0 || nh > (u32)NT) && act[j]) {
        const u32 n = s_cnt[key[j]];
        u32 p = kb[j];
        for (u32 k = 0; k < n; ++k) p += (s_tmp[kb[j] + k] < comp[j]) ? 1u : 0u;
        s_meta[p] = (hs[j] << 11) | (mp[j] >> 8);
        const float s0 = bflo(hsh[j].x), s1 = bfhi(hsh[j].x), s2 = bflo(hsh[j].y), s3 = bfhi(hsh[j].y);
        u32 at[8];
#pragma unroll
        for (int w = 0; w < 4; ++w) {
          float a[4];
#pragma unroll
          for (int u = 0; u < 4; ++u) {
            const f32x4 cv = *(const f32x4*)(s_cgt + w * 16 + u * 4);
            a[u] = cv.x * s0 + cv.y * s1 + cv.z * s2 + cv.w * s3;
          }
          at[2 * w] = cvtpk(a[0], a[1]);
          at[2 * w + 1] = cvtpk(a[2], a[3]);
        }
        u32x4* pa = (u32x4*)(smem + O_AT + p * 32);
        pa[0] = u32x4{at[0], at[1], at[2], at[3]};
        pa[1] = u32x4{at[4], at[5], at[6], at[7]};
        u32x4* pe = (u32x4*)(smem + O_EA + p * 32);
        pe[0] = hea[j][0];
        pe[1] = hea[j][1];
      }
    }
    if (hf == 0 && lc > 0) {
      const u32 p = ls + lc + lane;
      if (p < ls + ((lc + 7u) & ~7u)) {
        const u32 sl = (u32)(s_tmp[ls + lc - 1] >> 32);
        s_meta[p] = (sl << 11) | 15u;
        const u32x4 zz = {0u, 0u, 0u, 0u};
        u32x4* pe = (u32x4*)(smem + O_EA + p * 32);
        pe[0] = zz;
        pe[1] = zz;
      }
    }
    __syncthreads();

    f32x16 acc[4];
    if (first) {
#pragma unroll
      for (int r = 0; r < 4; ++r)
#pragma unroll
        for (int i = 0; i < 16; ++i) acc[r][i] = 0.f;
    } else {
      const f32x4* wsp = (const f32x4*)ws + ((size_t)blockIdx.x * NW + wv) * 1024 + lane;
#pragma unroll
      for (int r = 0; r < 4; ++r)
#pragma unroll
        for (int i = 0; i < 4; ++i) {
          const f32x4 t = wsp[(r * 4 + i) * 64];
          acc[r][4 * i] = t.x; acc[r][4 * i + 1] = t.y; acc[r][4 * i + 2] = t.z; acc[r][4 * i + 3] = t.w;
        }
    }
    const int ng = (int)((lc + 7u) >> 3);
    if (ng > 0) {
      MainCtx c;
      c.xb = x;
      c.xoff = hf * 256 + (lane & 31) * 8;
      c.myslot = (lane & 31) >> 2;
      c.atlane = O_AT + (lane & 3) * 8 + (ls + 4 * h) * 32;
      c.metaaddr = O_META + (ls + 4 * h) * 4;
      const bool match = (q4 >> 1) == ((c16 >> 2) & 1);
      c.eaaddr = match ? O_EA + (ls + 4 * (c16 >> 3) + (c16 & 3)) * 32 + 16 * (q4 & 1) : O_ZERO;
      c.eastride = match ? 256 : 0;
      c.wraddr = O_WR + hf * 4096 + lane * 16;
      u32x4 mc = *(const u32x4*)(smem + c.metaaddr);
      u32x4 eaf = *(const u32x4*)(smem + c.eaaddr);
      u32x2 xb0[8], xb1[8];
      load_block(c.xb, c.xoff, mc[0], mc[1], xb0);
      load_block(c.xb, c.xoff, mc[2], mc[3], xb1);
      int g = 0;
      for (; g < ng - 1; ++g) step<true>(smem, c, g, mc, eaf, xb0, xb1, acc);
      step<false>(smem, c, g, mc, eaf, xb0, xb1, acc);
    }
    if (end >= E) {

  u32x4 wf[8][2];
#pragma unroll
  for (int ks = 0; ks < 8; ++ks) { wf[ks][0] = u32x4{0u, 0u, 0u, 0u}; wf[ks][1] = u32x4{0u, 0u, 0u, 0u}; }
  if (wv < 8) {
    const int cb = 128 * (q4 & 1) + 64 * (q4 >> 1);
    const char* p = w_out + (size_t)cb * 512 + (wv * 32 + 2 * c16) * 2;
#pragma unroll
    for (int ks = 0; ks < 8; ++ks) {
      u32 L[8];
#pragma unroll
      for (int j = 0; j < 8; ++j) L[j] = *(const u32*)(p + (ks * 8 + j) * 512);
#pragma unroll
      for (int jj = 0; jj < 4; ++jj) {
        wf[ks][0][jj] = __builtin_amdgcn_perm(L[2 * jj + 1], L[2 * jj], 0x05040100u);
        wf[ks][1][jj] = __builtin_amdgcn_perm(L[2 * jj + 1], L[2 * jj], 0x07060302u);
      }
    }
  }
  {
    const u32 loc = s_inv[rg * 8 + ((lane & 31) >> 2)];
    if (loc != 0xffffffffu) {
      char* rowp = smem + O_TILE + (loc * 4 + (lane & 3)) * TROW + (hf * 128 + 16 * h) * 2;
#pragma unroll
      for (int qq = 0; qq < 4; ++qq) {
        u32 d[8];
#pragma unroll
        for (int a = 0; a < 4; ++a) {
          d[2 * a] = cvtpk(acc[0][4 * qq + a], acc[1][4 * qq + a]);
          d[2 * a + 1] = cvtpk(acc[2][4 * qq + a], acc[3][4 * qq + a]);
        }
        u32x4* pp = (u32x4*)(rowp + qq * 64);
        pp[0] = u32x4{d[0], d[1], d[2], d[3]};
        pp[1] = u32x4{d[4], d[5], d[6], d[7]};
      }
    }
  }
  __syncthreads();
  if (wv < 8) {
    const int cb = 128 * (q4 & 1) + 64 * (q4 >> 1);
    const char* tp = smem + O_TILE + c16 * TROW + cb * 2;
    char* op = out + ((size_t)n0 * 4 + c16) * 512 + (wv * 32 + 8 * q4) * 2;
    const u32 nrows = cntN * 4;
#pragma unroll 2
    for (int rb = 0; rb < MAXN * 4 / 16; ++rb) {
      f32x4 a0 = {0.f, 0.f, 0.f, 0.f}, a1 = {0.f, 0.f, 0.f, 0.f};
#pragma unroll
      for (int ks = 0; ks < 8; ++ks) {
        const u32x4 bf = *(const u32x4*)(tp + rb * 16 * TROW + ks * 16);
        a0 = MFMA16(__builtin_bit_cast(bf16x8, wf[ks][0]), __builtin_bit_cast(bf16x8, bf), a0);
        a1 = MFMA16(__builtin_bit_cast(bf16x8, wf[ks][1]), __builtin_bit_cast(bf16x8, bf), a1);
      }
      if ((u32)(rb * 16 + c16) < nrows) {
        const u32x4 o = {cvtpk(a0[0], a1[0]), cvtpk(a0[1], a1[1]), cvtpk(a0[2], a1[2]), cvtpk(a0[3], a1[3])};
        *(u32x4*)(op + (size_t)rb * 16 * 512) = o;
      }
    }
  }
  return;
    }
    int ln = tid0 & 63;
    asm volatile("" : "+v"(ln));
    f32x4* wsp = (f32x4*)ws + ((size_t)blockIdx.x * NW + wv) * 1024 + ln;
#pragma unroll
    for (int r = 0; r < 4; ++r)
#pragma unroll
      for (int i = 0; i < 4; ++i)
        wsp[(r * 4 + i) * 64] = f32x4{acc[r][4 * i], acc[r][4 * i + 1], acc[r][4 * i + 2], acc[r][4 * i + 3]};
    pos = end;
    first = false;
  }
}

}

namespace fast {
constexpr int ANT = 640;
constexpr int BNT = 512;
constexpr int SEGW = 128;
constexpr int LCAP = 8 * SEGW + 32;
constexpr int TROW = 528;
constexpr int MAXE = 256 * ANT;
#define PF1 0
#define PF2 0
constexpr size_t W_TAB = 0;
constexpr size_t W_WR = W_TAB + 256 * 256 * 4;
constexpr size_t W_WO = W_WR + 8192;
constexpr size_t W_M = W_WO + 131072;
constexpr size_t W_P = W_M + (size_t)MAXE * 8;
constexpr size_t W_END = W_P + (size_t)MAXE * 64;
constexpr int O_CNT = 0;
constexpr int O_BASE = O_CNT + 1024;
constexpr int O_DEG = O_BASE + 1024;
constexpr int O_RANK = O_DEG + 256;
constexpr int O_INV = O_RANK + 256;
constexpr int O_ZERO = O_INV + 256;
constexpr int O_MISC = O_ZERO + 64;
constexpr int O_META = O_MISC + 64;
constexpr int O_AT = O_META + LCAP * 4;
constexpr int O_EA = O_AT + LCAP * 32;
constexpr int O_TILE = O_EA + LCAP * 32;
constexpr int O_EXP = O_TILE;
constexpr int O_POS = O_EXP + 8 * SEGW * 4;
constexpr int O_TMP = O_POS + 8 * SEGW * 4;
constexpr int LDS_BYTES = O_TILE + 160 * TROW;
static_assert(LDS_BYTES <= 163840, "LDS");
static_assert((O_AT % 16) == 0 && (O_EA % 16) == 0 && (O_TILE % 16) == 0 && (O_META % 16) == 0, "align");

#define DPPADD(t, ctrl, rmask, bc) t += (u32)__builtin_amdgcn_update_dpp(0, (int)(t), ctrl, rmask, 0xf, bc)
DI u32 scan16(u32 t) {
  DPPADD(t, 0x111, 0xf, true);
  DPPADD(t, 0x112, 0xf, true);
  DPPADD(t, 0x114, 0xf, true);
  DPPADD(t, 0x118, 0xf, true);
  return t;
}
DI u32 scan32(u32 t) { t = scan16(t); DPPADD(t, 0x142, 0xa, false); return t; }
DI u32 scan64(u32 t) { t = scan32(t); DPPADD(t, 0x143, 0xc, false); return t; }
DI void wavesync() { __builtin_amdgcn_fence(__ATOMIC_RELEASE, "wavefront"); __builtin_amdgcn_wave_barrier(); __builtin_amdgcn_fence(__ATOMIC_ACQUIRE, "wavefront"); }

__global__ __launch_bounds__(ANT) void bin_kernel(
    const int* __restrict__ src, const int* __restrict__ dst, const char* __restrict__ sh,
    const char* __restrict__ eattr, const u32* __restrict__ cg2, const unsigned short* __restrict__ w_r,
    const unsigned short* __restrict__ w_out, char* __restrict__ ws, int N, int E, int S, float invN) {
  __shared__ __attribute__((aligned(16))) u32 s_cnt[256];
  __shared__ __attribute__((aligned(16))) u32 s_base[256];
  __shared__ __attribute__((aligned(16))) u32x2 s_m[ANT];
  __shared__ __attribute__((aligned(16))) u32x4 s_p[ANT * 4];
  const int tid0 = threadIdx.x, tid = tid0, lane = tid & 63;
  const int wv = __builtin_amdgcn_readfirstlane(tid >> 6);
  const int i = blockIdx.x;
  ST(0);
  int ne = E - i * S;
  ne = ne < 0 ? 0 : (ne > S ? S : ne);
  const bool act = tid < ne;
  u32 dn = 0, sn = 0;
  u32x2 shv = {0u, 0u};
  u32x4 e0 = {0u, 0u, 0u, 0u}, e1 = {0u, 0u, 0u, 0u};
  const u32 e = (u32)(i * S + tid);
  if (act) {
    dn = (u32)dst[e];
    sn = (u32)src[e];
    shv = *(const u32x2*)(sh + (size_t)e * 8);
    e0 = *(const u32x4*)(eattr + (size_t)e * 32);
    e1 = *(const u32x4*)(eattr + (size_t)e * 32 + 16);
  }
  if (tid < 256) s_cnt[tid] = 0u;
  if (wv == 9 && lane < 34) {
    const unsigned short* p;
    char* o;
    if (lane < 32) {
      const int id = i * 32 + lane;
      const int l2 = id & 63, h = (id >> 6) & 1, ks = (id >> 7) & 7, w8 = id >> 10;
      const int q4 = l2 >> 4, c16 = l2 & 15;
      p = w_out + (128 * (q4 & 1) + 64 * (q4 >> 1) + 8 * ks) * 256 + w8 * 32 + 2 * c16 + h;
      o = ws + W_WO + (size_t)id * 16;
    } else {
      const int id = i * 2 + (lane - 32);
      const int l2 = id & 63, t = (id >> 6) & 3, hf = id >> 8;
      const int kg = l2 >> 4, j = l2 & 15;
      p = w_r + (8 * (kg & 1)) * 256 + hf * 128 + 8 * j + 2 * t + (kg >> 1);
      o = ws + W_WR + (size_t)id * 16;
    }
    u32 v[8];
#pragma unroll
    for (int k = 0; k < 8; ++k) v[k] = p[k * 256];
    *(u32x4*)o = u32x4{v[0] | (v[1] << 16), v[2] | (v[3] << 16), v[4] | (v[5] << 16), v[6] | (v[7] << 16)};
  }
  __syncthreads();
  u32 b = (u32)((float)(dn * 256u + 255u) * invN);
  b = umin(b, 255u);
  if ((((b + 1u) * (u32)N) >> 8) <= dn) b += 1u;
  if (((b * (u32)N) >> 8) > dn) b -= 1u;
  b = umin(b, 255u);
  const u32 loc = dn - ((b * (u32)N) >> 8);
  u32 rk = 0;
  if (act) rk = atomicAdd(&s_cnt[b], 1u);
  u32 at[8];
  {
    const float s0 = bflo(shv.x), s1 = bfhi(shv.x), s2 = bflo(shv.y), s3 = bfhi(shv.y);
#pragma unroll
    for (int w = 0; w < 4; ++w) {
      float a[4];
#pragma unroll
      for (int u = 0; u < 4; ++u) {
        const u32 c0 = cg2[u * 8 + 0 + (w >> 1)], c1 = cg2[u * 8 + 2 + (w >> 1)];
        const u32 c2 = cg2[u * 8 + 4 + (w >> 1)], c3 = cg2[u * 8 + 6 + (w >> 1)];
        a[u] = ((w & 1) ? bfhi(c0) : bflo(c0)) * s0 + ((w & 1) ? bfhi(c1) : bflo(c1)) * s1 +
               ((w & 1) ? bfhi(c2) : bflo(c2)) * s2 + ((w & 1) ? bfhi(c3) : bflo(c3)) * s3;
      }
      at[2 * w] = cvtpk(a[0], a[1]);
      at[2 * w + 1] = cvtpk(a[2], a[3]);
    }
  }
  __syncthreads();
  {
    const u32x4 c = *(const u32x4*)(s_cnt + 4 * lane);
    const u32 p1 = c.x, p2 = p1 + c.y, p3 = p2 + c.z, tot = p3 + c.w;
    const u32 ex = scan64(tot) - tot;
    const u32x4 bs = {ex, ex + p1, ex + p2, ex + p3};
    *(u32x4*)(s_base + 4 * lane) = bs;
    if (wv < 4) {
      const u32 bb = wv == 0 ? bs.x : wv == 1 ? bs.y : wv == 2 ? bs.z : bs.w;
      const u32 cc = wv == 0 ? c.x : wv == 1 ? c.y : wv == 2 ? c.z : c.w;
      ((u32*)(ws + W_TAB))[(4 * lane + wv) * 256 + (i & 7) * 32 + (i >> 3)] = bb | (cc << 16);
    }
    wavesync();
  }
  if (act) {
    const u32 pos = s_base[b] + rk;
    s_m[pos] = u32x2{sn, (e << 6) | loc};
    s_p[pos * 4 + 0] = u32x4{at[0], at[1], at[2], at[3]};
    s_p[pos * 4 + 1] = u32x4{at[4], at[5], at[6], at[7]};
    s_p[pos * 4 + 2] = e0;
    s_p[pos * 4 + 3] = e1;
  }
  __syncthreads();
  if (act) ((u32x2*)(ws + W_M))[e] = s_m[tid];
  u32x4* po = (u32x4*)(ws + W_P) + (size_t)i * S * 4;
#pragma unroll
  for (int k = 0; k < 4; ++k) {
    const int id = k * ANT + tid;
    if (id < ne * 4) po[id] = s_p[id];
  }
  ST(1);
}

struct Ctx {
  const char* xb;
  u32 xoff;
  u32 js;
  u32 atl;
  u32 mad;
  u32 ead, east;
};

DI void load_x(const Ctx& c, u32x2 m, u32x4 (&X)[8]) {
#pragma unroll
  for (int i = 0; i < 2; ++i) {
    const char* p = c.xb + (size_t)((m[i] & 0xfffff800u) | c.xoff);
#pragma unroll
    for (int u = 0; u < 4; ++u) X[4 * i + u] = *(const u32x4*)(p + u * 512);
  }
}

template <bool PF>
DI void step(char* smem, const Ctx& c, int g, u32x2& m0, u32x2& m1, const u32x4 (&wrf)[4], f32x4 (&rw)[4],
             const u32x4 (&X)[8], u32x4 (&Y)[8], f32x4 (&acc)[8][3]) {
  u32x4 bt[3];
  const u32 ab = c.atl + g * 256;
#pragma unroll
  for (int i = 0; i < 2; ++i) {
    const u32 d = (m0[i] & 15u) - c.js;
#pragma unroll
    for (int cb = 0; cb < 3; ++cb) {
      const u32 a = (d == 4u * cb) ? ab + i * 32 : (u32)O_ZERO;
      const u32x2 t = *(const u32x2*)(smem + a);
      bt[cb][2 * i] = t.x;
      bt[cb][2 * i + 1] = t.y;
    }
  }
  u32x4 eaf = {0u, 0u, 0u, 0u};
  if (PF) {
    eaf = *(const u32x4*)(smem + c.ead + (g + 1) * c.east);
    asm volatile("" ::"v"(X[0]), "v"(X[1]), "v"(X[2]), "v"(X[3]), "v"(X[4]), "v"(X[5]), "v"(X[6]), "v"(X[7]));
    load_x(c, m1, Y);
    m0 = m1;
    m1 = *(const u32x2*)(smem + c.mad + (g + 2) * 32);
    __builtin_amdgcn_sched_barrier(0);
  }
  const f32x4 z4 = {0.f, 0.f, 0.f, 0.f};
#pragma unroll
  for (int r = 0; r < 8; ++r) {
    u32x4 z;
#pragma unroll
    for (int i = 0; i < 2; ++i) {
      const float s = rw[r >> 1][2 * (r & 1) + i];
#pragma unroll
      for (int up = 0; up < 2; ++up) {
        const u32 a = X[4 * i + 2 * up][r >> 1], b = X[4 * i + 2 * up + 1][r >> 1];
        z[2 * i + up] = (r & 1) ? cvtpk(bfhi(a) * s, bfhi(b) * s) : cvtpk(bflo(a) * s, bflo(b) * s);
      }
    }
#pragma unroll
    for (int cb = 0; cb < 3; ++cb)
      acc[r][cb] = MFMA16(__builtin_bit_cast(bf16x8, z), __builtin_bit_cast(bf16x8, bt[cb]), acc[r][cb]);
    if (PF && (r & 1))
      rw[r >> 1] = MFMA16(__builtin_bit_cast(bf16x8, eaf), __builtin_bit_cast(bf16x8, wrf[r >> 1]), z4);
  }
}

__global__ __launch_bounds__(BNT, 2) void agg_kernel(const char* __restrict__ x, char* __restrict__ out,
                                                     char* __restrict__ ws, int N, int S, float bscale) {
  __shared__ __attribute__((aligned(16))) char smem[LDS_BYTES];
  const int tid0 = threadIdx.x;
  const int wv = __builtin_amdgcn_readfirstlane(tid0 >> 6);
  const int rg = wv >> 1, hf = wv & 1;
  const int b = blockIdx.x;
  const u32 n0 = (u32)(b * N) >> 8;
  const u32 cntN = ((u32)((b + 1) * N) >> 8) - n0;
  u32* s_cnt = (u32*)(smem + O_CNT);
  u32* s_base = (u32*)(smem + O_BASE);
  u32* s_deg = (u32*)(smem + O_DEG);
  u32* s_rank = (u32*)(smem + O_RANK);
  u32* s_inv = (u32*)(smem + O_INV);
  u32* s_misc = (u32*)(smem + O_MISC);
  u32* s_meta = (u32*)(smem + O_META);
  u32* s_exp = (u32*)(smem + O_EXP) + wv * SEGW;
  u32* s_pos = (u32*)(smem + O_POS) + wv * SEGW;
  u32* s_tmp = (u32*)(smem + O_TMP);
  ST(2);

  u32 te, pf1 = 0;
  {
    const int lane = tid0 & 63;
    te = ((const u32*)(ws + W_TAB))[b * 256 + wv * 32 + (lane & 31)];
    const u32 line = ((u32)wv * 32u + (u32)(b >> 3)) * 64u + (u32)lane;
    pf1 = *(const u32*)(x + (size_t)((tid0 < PF1 && line < (u32)N * 16u) ? line : 0u) * 128);
    if (tid0 >= 256 && tid0 < 256 + 128) s_deg[tid0 - 256] = 0u;
    if (tid0 >= 384 && tid0 < 384 + 64) s_inv[tid0 - 384] = 0xffffffffu;
    if (tid0 >= 448 && tid0 < 448 + 32) ((u32*)(smem + O_ZERO))[tid0 - 448] = 0u;
  }

  f32x4 acc[8][3];
#pragma unroll
  for (int r = 0; r < 8; ++r)
#pragma unroll
    for (int cb = 0; cb < 3; ++cb) acc[r][cb] = f32x4{0.f, 0.f, 0.f, 0.f};

  int nseg = 1;
  for (int seg = 0;; ++seg) {
    int tid = tid0;
    asm volatile("" : "+v"(tid));
    const int lane = tid & 63;
    const int q4 = lane >> 4, c16 = lane & 15;
    const int sb = seg * SEGW;
    u32x4 wrf[4];
#pragma unroll
    for (int t = 0; t < 4; ++t) wrf[t] = *(const u32x4*)(ws + W_WR + ((hf * 4 + t) * 64 + lane) * 16);
    if (tid < 256) s_cnt[tid] = 0u;
    asm volatile("" : "+v"(te));
    const int tc = (int)(te >> 16);
    const int tinc = (int)scan32((u32)tc);
    const int T = __builtin_amdgcn_readlane(tinc, 31);
    const int to = tinc - tc;
    const u32 g0 = (u32)((((lane & 31) << 3) | wv) * S) + (te & 0xffffu);
    ST(3);
    {
      int k = (sb - to > 0 ? sb - to : 0) + (lane >> 5);
      const int ke = (sb + SEGW - to < tc) ? sb + SEGW - to : tc;
#pragma clang loop unroll(disable) vectorize(disable) interleave(disable)
      for (; k < ke; k += 2) s_exp[to + k - sb] = g0 + (u32)k;
    }
    int Tn = T - sb;
    Tn = Tn < 0 ? 0 : (Tn > SEGW ? SEGW : Tn);
    wavesync();
    u32x2 mt[2];
    u32x4 pl[8];
    bool act[2];
    u32 gi[10];
#pragma unroll
    for (int j = 0; j < 2; ++j) gi[j] = s_exp[j * 64 + lane];
#pragma unroll
    for (int k = 0; k < 8; ++k) gi[2 + k] = s_exp[k * 16 + (lane >> 2)];
#pragma unroll
    for (int k = 0; k < 10; ++k) asm volatile("" : "+v"(gi[k]));
#pragma unroll
    for (int j = 0; j < 2; ++j) {
      act[j] = j * 64 + lane < Tn;
      mt[j] = ((const u32x2*)(ws + W_M))[act[j] ? gi[j] : 0u];
    }
#pragma unroll
    for (int k = 0; k < 8; ++k) {
      const int r = k * 16 + (lane >> 2);
      pl[k] = ((const u32x4*)(ws + W_P))[(r < Tn ? gi[2 + k] * 4u : 0u) + (lane & 3)];
    }
    u32 pf2[PF2 / BNT + 1];
#pragma unroll
    for (int k = 0; k < PF2 / BNT; ++k) {
      const u32 line = ((u32)(PF1 / 64 + k * 8 + wv) * 32u + (u32)(b >> 3)) * 64u + (u32)lane;
      pf2[k] = *(const u32*)(x + (size_t)((seg == 0 && line < (u32)N * 16u) ? line : 0u) * 128);
    }
    ST(4);
    __syncthreads();
    if (seg == 0) {
      if (lane == 0) atomicMax(&s_misc[0], (u32)T);
#pragma unroll
      for (int j = 0; j < 2; ++j)
        if (act[j]) atomicAdd(&s_deg[mt[j].y & 63u], 1u);
      ST(5);
      __syncthreads();
      const u32 d = s_deg[lane];
      u32 part = 0;
#pragma unroll
      for (int jj = 0; jj < 5; ++jj) {
        const int j = wv * 5 + jj;
        const u32 dj = __builtin_amdgcn_readlane(d, j);
        part += (dj > d || (dj == d && j < lane)) ? 1u : 0u;
      }
      if (lane < 40) atomicAdd(&s_rank[lane], part);
      nseg = (int)((__builtin_amdgcn_readfirstlane(s_misc[0]) + SEGW - 1) / SEGW);
      __syncthreads();
      if (wv == 0 && (u32)lane < cntN) {
        const u32 rk = s_rank[lane], slot = rk >> 2, r4 = rk & 3u;
        s_inv[((slot & 1u) ? 3u - r4 : r4) * 16u + slot] = (u32)lane;
      }
    }
    ST(6);
    u32 key[2], rk[2], slot[2], comp[2];
#pragma unroll
    for (int j = 0; j < 2; ++j) {
      key[j] = 0u; rk[j] = 0u; slot[j] = 0u; comp[j] = 0u;
      if (j * 64 < Tn) {
        const u32 nr = s_rank[mt[j].y & 63u];
        slot[j] = nr >> 2;
        const u32 r4 = nr & 3u;
        const u32 bk = umin((u32)((float)mt[j].x * bscale), 63u);
        key[j] = ((slot[j] & 1u) ? 3u - r4 : r4) * 64u + bk;
        comp[j] = (mt[j].x << 18) | (mt[j].y >> 6);
        if (act[j]) rk[j] = atomicAdd(&s_cnt[key[j]], 1u);
      }
    }
    __syncthreads();
    ST(7);
    u32 ls = 0, lc = 0;
    {
      const u32x4 c = *(const u32x4*)(s_cnt + 4 * lane);
      const u32 p1 = c.x, p2 = p1 + c.y, p3 = p2 + c.z, tot = p3 + c.w;
      const u32 inc = scan16(tot);
      u32 run = 0, st = 0;
#pragma unroll
      for (int k = 0; k < 4; ++k) {
        const u32 ck = __builtin_amdgcn_readlane(inc, 16 * k + 15);
        ls = (rg == k) ? run : ls;
        lc = (rg == k) ? ck : lc;
        st = (q4 == k) ? run : st;
        run += ck ? ((ck + 7u) & ~7u) : 8u;
      }
      const u32 ex = st + inc - tot;
      *(u32x4*)(s_base + 4 * lane) = u32x4{ex, ex + p1, ex + p2, ex + p3};
      wavesync();
    }
    u32 kb[2];
#pragma unroll
    for (int j = 0; j < 2; ++j) {
      kb[j] = 0u;
      if (j * 64 < Tn) {
        kb[j] = s_base[key[j]];
        if (act[j]) s_tmp[kb[j] + rk[j]] = comp[j];
      }
    }
    __syncthreads();
    ST(8);
#pragma unroll
    for (int j = 0; j < 2; ++j) {
      if (j * 64 < Tn && act[j]) {
        const u32 n = s_cnt[key[j]];
        u32 p = kb[j];
#pragma clang loop unroll(disable) vectorize(disable) interleave(disable)
        for (u32 k = 0; k < n; k += 4) {
          const u32 t0 = s_tmp[kb[j] + k], t1 = s_tmp[kb[j] + k + 1], t2 = s_tmp[kb[j] + k + 2], t3 = s_tmp[kb[j] + k + 3];
          p += (t0 < comp[j]) ? 1u : 0u;
          p += (k + 1 < n && t1 < comp[j]) ? 1u : 0u;
          p += (k + 2 < n && t2 < comp[j]) ? 1u : 0u;
          p += (k + 3 < n && t3 < comp[j]) ? 1u : 0u;
        }
        s_meta[p] = (mt[j].x << 11) | slot[j];
        s_pos[j * 64 + lane] = p;
      }
    }
    wavesync();
#pragma unroll
    for (int k = 0; k < 8; ++k) {
      const int r = k * 16 + (lane >> 2);
      if (k * 16 < Tn && r < Tn) {
        const u32 p = s_pos[r];
        *(u32x4*)(smem + ((lane & 2) ? O_EA : O_AT) + p * 32 + (lane & 1) * 16) = pl[k];
      }
    }
    const u32 lp = lc ? ((lc + 7u) & ~7u) : 8u;
    if (hf == 0 && lane < 8 && lc + lane < lp) {
      const u32 p = ls + lc + lane;
      const u32 sl = lc ? (s_tmp[ls + lc - 1] >> 18) : 0u;
      s_meta[p] = (sl << 11) | 15u;
      const u32x4 zz = {0u, 0u, 0u, 0u};
      u32x4* pe = (u32x4*)(smem + O_EA + p * 32);
      pe[0] = zz;
      pe[1] = zz;
    }
    __syncthreads();
    ST(9);

    u32x4 wf[8][2];
    {
      const int ng = (int)(lp >> 3);
      Ctx c;
      c.xb = x;
      c.xoff = hf * 256 + c16 * 16;
      c.js = c16 >> 2;
      c.atl = O_AT + (ls + 2 * q4) * 32 + (c16 & 3) * 8;
      c.mad = O_META + (ls + 2 * q4) * 4;
      const bool match = (q4 >> 1) == ((c16 >> 1) & 1);
      c.ead = match ? O_EA + (ls + 2 * (c16 >> 2) + (c16 & 1)) * 32 + 16 * (q4 & 1) : O_ZERO;
      c.east = match ? 256 : 0;
      u32x2 m0 = *(const u32x2*)(smem + c.mad);
      u32x2 m1 = *(const u32x2*)(smem + c.mad + 32);
      u32x4 XA[8], XB[8];
      load_x(c, m0, XA);
      f32x4 rw[4];
      {
        const u32x4 eaf = *(const u32x4*)(smem + c.ead);
        const f32x4 z4 = {0.f, 0.f, 0.f, 0.f};
#pragma unroll
        for (int t = 0; t < 4; ++t)
          rw[t] = MFMA16(__builtin_bit_cast(bf16x8, eaf), __builtin_bit_cast(bf16x8, wrf[t]), z4);
      }
      asm volatile("" ::"v"(pf1));
#pragma unroll
      for (int k = 0; k < PF2 / BNT; ++k) asm volatile("" ::"v"(pf2[k]));
      int g = 0;
      for (; g + 2 < ng; g += 2) {
        step<true>(smem, c, g, m0, m1, wrf, rw, XA, XB, acc);
        step<true>(smem, c, g + 1, m0, m1, wrf, rw, XB, XA, acc);
      }
      if (g + 1 < ng) {
        step<true>(smem, c, g, m0, m1, wrf, rw, XA, XB, acc);
        ++g;
#pragma unroll
        for (int k = 0; k < 8; ++k) XA[k] = XB[k];
      }
      {
        const char* wp = ws + W_WO + (size_t)(wv * 16 * 64 + lane) * 16;
#pragma unroll
        for (int ks = 0; ks < 8; ++ks) {
          wf[ks][0] = *(const u32x4*)(wp + (ks * 2 + 0) * 1024);
          wf[ks][1] = *(const u32x4*)(wp + (ks * 2 + 1) * 1024);
        }
        __builtin_amdgcn_sched_barrier(0);
      }
      step<false>(smem, c, g, m0, m1, wrf, rw, XA, XB, acc);
    }
    ST(10);
    if (seg + 1 < nseg) { __syncthreads(); continue; }

#pragma unroll
    for (int cb = 0; cb < 3; ++cb) {
      const u32 loc = s_inv[rg * 16 + cb * 4 + (c16 >> 2)];
      if (loc != 0xffffffffu) {
        u32x4* rowp = (u32x4*)(smem + O_TILE + (loc * 4 + (c16 & 3)) * TROW + (hf * 128 + 32 * q4) * 2);
#pragma unroll
        for (int a = 0; a < 4; ++a)
          rowp[a] = u32x4{cvtpk(acc[0][cb][a], acc[1][cb][a]), cvtpk(acc[2][cb][a], acc[3][cb][a]),
                          cvtpk(acc[4][cb][a], acc[5][cb][a]), cvtpk(acc[6][cb][a], acc[7][cb][a])};
      }
    }
    ST(11);
    __syncthreads();
    ST(12);
    {
      const int cb = 128 * (q4 & 1) + 64 * (q4 >> 1);
      const char* tp = smem + O_TILE + c16 * TROW + cb * 2;
      char* op = out + ((size_t)n0 * 4 + c16) * 512 + (wv * 32 + 8 * q4) * 2;
      const u32 nrows = cntN * 4;
#pragma unroll 2
      for (int rb = 0; rb < 10; ++rb) {
        f32x4 a0 = {0.f, 0.f, 0.f, 0.f}, a1 = {0.f, 0.f, 0.f, 0.f};
#pragma unroll
        for (int ks = 0; ks < 8; ++ks) {
          const u32x4 bf = *(const u32x4*)(tp + rb * 16 * TROW + ks * 16);
          a0 = MFMA16(__builtin_bit_cast(bf16x8, wf[ks][0]), __builtin_bit_cast(bf16x8, bf), a0);
          a1 = MFMA16(__builtin_bit_cast(bf16x8, wf[ks][1]), __builtin_bit_cast(bf16x8, bf), a1);
        }
        if ((u32)(rb * 16 + c16) < nrows) {
          const u32x4 o = {cvtpk(a0[0], a1[0]), cvtpk(a0[1], a1[1]), cvtpk(a0[2], a1[2]), cvtpk(a0[3], a1[3])};
          *(u32x4*)(op + (size_t)rb * 16 * 512) = o;
        }
      }
    }
    ST(13);
    return;
  }
}
}

extern "C" void kernel_launch(void* const* d_in, const int* in_sizes, int n_in, void* d_out, int out_size,
                              void* d_ws, size_t ws_size, hipStream_t stream) {
  const int N = in_sizes[0] / 1024;
  const int E = in_sizes[1];
  if (N <= 0) return;
  if (N <= 10240 && E <= fast::MAXE && ws_size >= fast::W_END) {
    int S = (E + 255) / 256;
    if (S < 1) S = 1;
    fast::bin_kernel<<<256, fast::ANT, 0, stream>>>(
        (const int*)d_in[1], (const int*)d_in[2], (const char*)d_in[3], (const char*)d_in[4], (const u32*)d_in[5],
        (const unsigned short*)d_in[6], (const unsigned short*)d_in[7], (char*)d_ws, N, E, S, 1.0f / (float)N);
    fast::agg_kernel<<<256, fast::BNT, 0, stream>>>((const char*)d_in[0], (char*)d_out, (char*)d_ws, N, S,
                                                    64.0f / (float)N);
    return;
  }
  int G = (N + gen::MAXN - 1) / gen::MAXN;
  if (G < 256) G = 256;
  if (G > N) G = N;
  size_t gmax = ws_size / ((size_t)gen::NW * 16384);
  if (gmax > 65536) gmax = 65536;
  if (gmax < 1) return;
  for (int b0 = 0; b0 < G; b0 += (int)gmax) {
    const int g = (G - b0 < (int)gmax) ? G - b0 : (int)gmax;
    gen::mace_kernel<<<g, gen::NT, 0, stream>>>(
        (const char*)d_in[0], (const int*)d_in[1], (const int*)d_in[2], (const char*)d_in[3], (const char*)d_in[4],
        (const unsigned short*)d_in[5], (const char*)d_in[6], (const char*)d_in[7], (char*)d_out, (float*)d_ws, N, E,
        b0, G, (float)gen::NB / (float)N);
  }
}
```

```cpp
#include <hip/hip_runtime.h>
#include <hip/hip_bf16.h>
#include <stdint.h>

typedef unsigned int u32;
typedef unsigned long long u64;
typedef __attribute__((ext_vector_type(8))) short bf16x8;
typedef __attribute__((ext_vector_type(2))) __bf16 bf16x2_t;
typedef __attribute__((ext_vector_type(2))) float f32x2;
typedef __attribute__((ext_vector_type(4))) float f32x4;
typedef __attribute__((ext_vector_type(16))) float f32x16;
typedef __attribute__((ext_vector_type(2))) unsigned int u32x2;
typedef __attribute__((ext_vector_type(4))) unsigned int u32x4;

#define DI __device__ __forceinline__
#define ST(k) do { } while (0)
#define MFMA32(a, b, c) __builtin_amdgcn_mfma_f32_32x32x16_bf16((a), (b), (c), 0, 0, 0)
#define MFMA16(a, b, c) __builtin_amdgcn_mfma_f32_16x16x32_bf16((a), (b), (c), 0, 0, 0)

DI u32 cvtpk(float a, float b) {
  f32x2 v = {a, b};
  bf16x2_t r = __builtin_convertvector(v, bf16x2_t);
  return __builtin_bit_cast(u32, r);
}
DI float bflo(u32 v) { return __builtin_bit_cast(float, v << 16); }
DI float bfhi(u32 v) { return __builtin_bit_cast(float, v & 0xffff0000u); }
DI u32 mbcnt(u64 m) { return __builtin_amdgcn_mbcnt_hi((u32)(m >> 32), __builtin_amdgcn_mbcnt_lo((u32)m, 0u)); }
DI u32 umin(u32 a, u32 b) { return a < b ? a : b; }

namespace gen {
constexpr int NT = 768, NW = 12, NRG = 6, NB = 32;
constexpr int MAXN = 40;
constexpr int CAP = 944;
constexpr int LCAP = CAP + 48;
constexpr int CAPW = 128;
constexpr int WMIN = 512;
constexpr int SU = 9;
constexpr int TROW = 528;
constexpr int PFROWS = 1536;

constexpr int O_CNT = 0;
constexpr int O_BASE = O_CNT + 192 * 4;
constexpr int O_DEG = O_BASE + 192 * 4;
constexpr int O_MAP = O_DEG + 64 * 4;
constexpr int O_INV = O_MAP + 64 * 4;
constexpr int O_CGT = O_INV + 64 * 4;
constexpr int O_ZERO = O_CGT + 64 * 4;
constexpr int O_MISC = O_ZERO + 64;
constexpr int O_WR = O_MISC + 64;
constexpr int O_META = O_WR + 8192;
constexpr int O_AT = O_META + LCAP * 4;
constexpr int O_EA = O_AT + LCAP * 32;
constexpr int O_TILE = O_EA + LCAP * 32;
constexpr int O_CANDQ = O_TILE;
constexpr int O_CANDI = O_CANDQ + NW * CAPW * 16;
constexpr int O_HITE = O_CANDI + NW * CAPW * 4;
constexpr int O_HITL = O_HITE + 1024 * 4;
constexpr int O_TMP = O_HITL + 1024 * 4;
constexpr int LDS_BYTES = O_TILE + MAXN * 4 * TROW;
static_assert(O_TMP + LCAP * 8 <= LDS_BYTES, "alias region too small");
static_assert(LDS_BYTES <= 163840, "LDS");
static_assert((O_AT % 16) == 0 && (O_EA % 16) == 0 && (O_TILE % 16) == 0 && (O_WR % 16) == 0, "align");

struct MainCtx {
  const char* xb;
  u32 xoff;
  u32 myslot;
  u32 atlane;
  u32 metaaddr;
  u32 eaaddr, eastride;
  u32 wraddr;
};

DI void load_block(const char* xb, u32 xoff, u32 m0, u32 m1, u32x2 (&buf)[8]) {
  const u32 o0 = (m0 & 0xfffff800u) | xoff, o1 = (m1 & 0xfffff800u) | xoff;
  const char* p0 = xb + (size_t)o0;
  const char* p1 = xb + (size_t)o1;
#pragma unroll
  for (int u = 0; u < 4; ++u) buf[u] = *(const u32x2*)(p0 + u * 512);
#pragma unroll
  for (int u = 0; u < 4; ++u) buf[4 + u] = *(const u32x2*)(p1 + u * 512);
}

DI u32x4 make_z(const u32x2 (&buf)[8], int r, float s0, float s1) {
  u32x4 z;
#pragma unroll
  for (int i = 0; i < 2; ++i) {
    const float s = i ? s1 : s0;
#pragma unroll
    for (int up = 0; up < 2; ++up) {
      const u32 a = (r & 2) ? buf[4 * i + 2 * up].y : buf[4 * i + 2 * up].x;
      const u32 b = (r & 2) ? buf[4 * i + 2 * up + 1].y : buf[4 * i + 2 * up + 1].x;
      z[2 * i + up] = (r & 1) ? cvtpk(bfhi(a) * s, bfhi(b) * s) : cvtpk(bflo(a) * s, bflo(b) * s);
    }
  }
  return z;
}

template <bool PF>
DI void step(char* smem, const MainCtx& c, int g, u32x4& mc, u32x4& eaf, u32x2 (&xb0)[8], u32x2 (&xb1)[8],
             f32x16 (&acc)[4]) {
  const u32 ab = c.atlane + g * 256;
  u32x2 bt[4];
#pragma unroll
  for (int k = 0; k < 4; ++k) {
    const u32 a = ((mc[k] & 15u) == c.myslot) ? ab + k * 32 : (u32)O_ZERO;
    bt[k] = *(const u32x2*)(smem + a);
  }
  if (PF) mc = *(const u32x4*)(smem + c.metaaddr + (g + 1) * 32);
  f32x4 rw[4];
  const f32x4 z4 = {0.f, 0.f, 0.f, 0.f};
  u32 wra = c.wraddr;
  asm volatile("" : "+v"(wra));
#pragma unroll
  for (int r = 0; r < 4; ++r) {
    const u32x4 wr = *(const u32x4*)(smem + wra + r * 1024);
    rw[r] = MFMA16(__builtin_bit_cast(bf16x8, eaf), __builtin_bit_cast(bf16x8, wr), z4);
  }
  if (PF) eaf = *(const u32x4*)(smem + c.eaaddr + (g + 1) * c.eastride);
  {
    const u32x4 b0 = {bt[0].x, bt[0].y, bt[1].x, bt[1].y};
#pragma unroll
    for (int r = 0; r < 4; ++r) {
      const u32x4 z = make_z(xb0, r, rw[r][0], rw[r][1]);
      acc[r] = MFMA32(__builtin_bit_cast(bf16x8, z), __builtin_bit_cast(bf16x8, b0), acc[r]);
    }
    if (PF) load_block(c.xb, c.xoff, mc[0], mc[1], xb0);
  }
  {
    const u32x4 b1 = {bt[2].x, bt[2].y, bt[3].x, bt[3].y};
#pragma unroll
    for (int r = 0; r < 4; ++r) {
      const u32x4 z = make_z(xb1, r, rw[r][2], rw[r][3]);
      acc[r] = MFMA32(__builtin_bit_cast(bf16x8, z), __builtin_bit_cast(bf16x8, b1), acc[r]);
    }
    if (PF) load_block(c.xb, c.xoff, mc[2], mc[3], xb1);
  }
}

__global__ __launch_bounds__(NT, 3) void mace_kernel(
    const char* __restrict__ x, const int* __restrict__ src, const int* __restrict__ dst,
    const char* __restrict__ sh, const char* __restrict__ eattr, const unsigned short* __restrict__ cg,
    const char* __restrict__ w_r, const char* __restrict__ w_out, char* __restrict__ out,
    float* __restrict__ ws, int N, int E, int b0, int G, float bscale) {
  __shared__ __attribute__((aligned(16))) char smem[LDS_BYTES];
  const int tid0 = threadIdx.x;
  const int wv = __builtin_amdgcn_readfirstlane(tid0 >> 6);
  const int rg = wv >> 1, hf = wv & 1;
  const int b = b0 + blockIdx.x;
  const u32 n0 = (u32)(((u64)b * (u64)N) / (u64)G);
  const u32 cntN = (u32)((((u64)b + 1) * (u64)N) / (u64)G) - n0;

  u32* s_cnt = (u32*)(smem + O_CNT);
  u32* s_base = (u32*)(smem + O_BASE);
  u32* s_deg = (u32*)(smem + O_DEG);
  u32* s_map = (u32*)(smem + O_MAP);
  u32* s_inv = (u32*)(smem + O_INV);
  float* s_cgt = (float*)(smem + O_CGT);
  u32* s_misc = (u32*)(smem + O_MISC);
  u32* s_hitE = (u32*)(smem + O_HITE);
  u32* s_hitL = (u32*)(smem + O_HITL);
  u64* s_tmp = (u64*)(smem + O_TMP);
  u32* s_meta = (u32*)(smem + O_META);

  if (wv < 2) {
    const int lane = tid0 & 63;
    const int q4 = lane >> 4, c16 = lane & 15;
    u32x4 wr[4];
    u32x2 L[8];
    const char* p = w_r + (size_t)(8 * (q4 & 1)) * 512 + (hf * 128 + 64 * (q4 >> 1) + 4 * c16) * 2;
#pragma unroll
    for (int j = 0; j < 8; ++j) L[j] = *(const u32x2*)(p + j * 512);
#pragma unroll
    for (int jj = 0; jj < 4; ++jj) {
      wr[0][jj] = __builtin_amdgcn_perm(L[2 * jj + 1].x, L[2 * jj].x, 0x05040100u);
      wr[1][jj] = __builtin_amdgcn_perm(L[2 * jj + 1].x, L[2 * jj].x, 0x07060302u);
      wr[2][jj] = __builtin_amdgcn_perm(L[2 * jj + 1].y, L[2 * jj].y, 0x05040100u);
      wr[3][jj] = __builtin_amdgcn_perm(L[2 * jj + 1].y, L[2 * jj].y, 0x07060302u);
    }
#pragma unroll
    for (int r = 0; r < 4; ++r) *(u32x4*)(smem + O_WR + hf * 4096 + r * 1024 + lane * 16) = wr[r];
  }
  if (wv == 2) {
    const int tid = tid0 & 63;
    const int w = tid >> 4, u = (tid >> 2) & 3, v = tid & 3;
    s_cgt[tid] = bflo((u32)cg[u * 16 + v * 4 + w]);
    s_map[tid] = (u32)(tid % NRG) | ((u32)(tid / NRG) << 8);
    const u32 l = (u32)((tid & 7) * NRG + (tid >> 3));
    s_inv[tid] = (tid < NRG * 8 && l < cntN) ? l : 0xffffffffu;
    if (tid < 16) ((u32*)(smem + O_ZERO))[tid] = 0u;
  }

  bool first = true, again = false;
  int pos = 0;
  int W = (E + 3) & ~3;
  for (;;) {
    if (again) __syncthreads();
    again = true;
    int tid = tid0;
    asm volatile("" : "+v"(tid));
    const int lane = tid & 63;
    const int q4 = lane >> 4, c16 = lane & 15, h = lane >> 5;
    u32 pfv = 0;
    {
      const u32 line = (u32)(b >> 3) * NT + tid;
      if (first && line < (u32)PFROWS * 16u && line < (u32)N * 16u) pfv = *(const u32*)(x + (size_t)line * 128);
    }
    if (tid < 192) s_cnt[tid] = 0u;
    if (tid >= 192 && tid < 256) s_deg[tid - 192] = 0u;
    if (tid >= 256 && tid < 258) s_misc[tid - 256] = 0u;
    __syncthreads();
    const int end = (E - pos < W) ? E : pos + W;

    {
      const u32x4* dq = (const u32x4*)dst;
      u32x4* cq = (u32x4*)(smem + O_CANDQ) + wv * CAPW;
      u32* ci = (u32*)(smem + O_CANDI) + wv * CAPW;
      const int q0 = pos >> 2, qf = end >> 2;
      u32 nc = 0;
      for (int qb = q0 + wv * 64; qb < qf; qb += NT * SU) {
        u32x4 v[SU];
#pragma unroll
        for (int k = 0; k < SU; ++k) {
          const int q = qb + k * NT + lane;
          v[k] = dq[q < qf ? q : qf - 1];
        }
#pragma unroll
        for (int k = 0; k < SU; ++k) {
          const int q = qb + k * NT + lane;
          const u32 m = umin(umin(v[k].x - n0, v[k].y - n0), umin(v[k].z - n0, v[k].w - n0));
          const bool hit = (m < cntN) && (q < qf);
          const u64 mask = __ballot(hit);
          if (mask) {
            const u32 p = nc + mbcnt(mask);
            if (hit && p < (u32)CAPW) { cq[p] = v[k]; ci[p] = (u32)q; }
            nc += (u32)__popcll(mask);
          }
        }
      }
      if (wv == NW - 1 && (end & 3)) {
        u32x4 v;
        const int e4 = qf * 4;
        v.x = (u32)dst[e4];
        v.y = (e4 + 1 < end) ? (u32)dst[e4 + 1] : n0 - 1u;
        v.z = (e4 + 2 < end) ? (u32)dst[e4 + 2] : n0 - 1u;
        v.w = n0 - 1u;
        if (lane == 0 && nc < (u32)CAPW) { cq[nc] = v; ci[nc] = (u32)qf; }
        nc += 1;
      }
      if (nc > (u32)CAPW) { if (lane == 0) s_misc[1] = 1u; nc = CAPW; }
      __builtin_amdgcn_wave_barrier();
      for (u32 i0 = 0; i0 < nc; i0 += 64) {
        const u32 i = i0 + lane;
        const bool valid = i < nc;
        const u32x4 v = cq[valid ? i : 0];
        const u32 q = ci[valid ? i : 0];
        const u32 t[4] = {v.x - n0, v.y - n0, v.z - n0, v.w - n0};
        u64 mk[4];
        u32 tot = 0;
#pragma unroll
        for (int k = 0; k < 4; ++k) { mk[k] = __ballot(valid && t[k] < cntN); tot += (u32)__popcll(mk[k]); }
        u32 bs = 0;
        if (lane == 0) bs = atomicAdd(&s_misc[0], tot);
        bs = __builtin_amdgcn_readfirstlane(bs);
#pragma unroll
        for (int k = 0; k < 4; ++k) {
          const u32 p = bs + mbcnt(mk[k]);
          if (valid && t[k] < cntN && p < (u32)CAP) {
            s_hitE[p] = 4 * q + k;
            s_hitL[p] = t[k];
            atomicAdd(&s_deg[t[k]], 1u);
          }
          bs += (u32)__popcll(mk[k]);
        }
      }
    }
    asm volatile("" ::"v"(pfv));
    __syncthreads();
    const u32 nh = __builtin_amdgcn_readfirstlane(s_misc[0]);
    if (nh > (u32)CAP || __builtin_amdgcn_readfirstlane(s_misc[1]) != 0u) {
      W = ((W >> 1) + 3) & ~3;
      if (W < WMIN) W = WMIN;
      continue;
    }

    u32 he[2], hl[2], hs[2];
    u32x2 hsh[2];
    u32x4 hea[2][2];
    bool act[2];
#pragma unroll
    for (int j = 0; j < 2; ++j) {
      const u32 i = tid + j * NT;
      act[j] = i < nh;
      he[j] = 0u; hl[j] = 0u; hs[j] = 0u;
      hsh[j] = u32x2{0u, 0u};
      hea[j][0] = u32x4{0u, 0u, 0u, 0u};
      hea[j][1] = u32x4{0u, 0u, 0u, 0u};
      if (j == 0 ? nh > 0u : nh > (u32)NT) {
        const u32 ii = act[j] ? i : 0;
        he[j] = s_hitE[ii];
        hl[j] = s_hitL[ii];
        hs[j] = (u32)src[he[j]];
        hsh[j] = *(const u32x2*)(sh + (size_t)he[j] * 8);
        hea[j][0] = *(const u32x4*)(eattr + (size_t)he[j] * 32);
        hea[j][1] = *(const u32x4*)(eattr + (size_t)he[j] * 32 + 16);
      }
    }
    if (first && wv == NW - 1) {
      const u32 d = s_deg[lane];
      u32 rank = 0;
      for (u32 j = 0; j < cntN; ++j) {
        const u32 dj = __builtin_amdgcn_readlane(d, j);
        rank += (dj > d || (dj == d && j < (u32)lane)) ? 1u : 0u;
      }
      const u32 slot = rank / NRG, r = rank % NRG;
      const u32 g = (slot & 1) ? (NRG - 1 - r) : r;
      s_inv[lane] = 0xffffffffu;
      __builtin_amdgcn_wave_barrier();
      if ((u32)lane < cntN) { s_map[lane] = g | (slot << 8); s_inv[g * 8 + slot] = lane; }
    }
    __syncthreads();

    u32 key[2], rk[2], mp[2];
#pragma unroll
    for (int j = 0; j < 2; ++j) {
      key[j] = 0u; rk[j] = 0u; mp[j] = 0u;
      if (j == 0 ? nh > 0u : nh > (u32)NT) {
        mp[j] = s_map[hl[j]];
        u32 bk = (u32)((float)hs[j] * bscale);
        bk = umin(bk, NB - 1);
        key[j] = (mp[j] & 255u) * NB + bk;
        if (act[j]) rk[j] = atomicAdd(&s_cnt[key[j]], 1u);
      }
    }
    __syncthreads();
    u32 ls = 0, lc = 0;
    {
      u32 v[3], inc[3];
#pragma unroll
      for (int k = 0; k < 3; ++k) {
        v[k] = s_cnt[k * 64 + lane];
        u32 t = v[k];
        t += (u32)__builtin_amdgcn_update_dpp(0, (int)t, 0x111, 0xf, 0xf, true);
        t += (u32)__builtin_amdgcn_update_dpp(0, (int)t, 0x112, 0xf, 0xf, true);
        t += (u32)__builtin_amdgcn_update_dpp(0, (int)t, 0x114, 0xf, 0xf, true);
        t += (u32)__builtin_amdgcn_update_dpp(0, (int)t, 0x118, 0xf, 0xf, true);
        t += (u32)__builtin_amdgcn_update_dpp(0, (int)t, 0x142, 0xa, 0xf, false);
        inc[k] = t;
      }
      u32 run = 0;
#pragma unroll
      for (int k = 0; k < 3; ++k) {
        const u32 c0 = __builtin_amdgcn_readlane(inc[k], 31);
        const u32 c1 = __builtin_amdgcn_readlane(inc[k], 63);
        const u32 s0 = run, s1 = run + ((c0 + 7u) & ~7u);
        run = s1 + ((c1 + 7u) & ~7u);
        ls = (rg == 2 * k) ? s0 : ls;
        lc = (rg == 2 * k) ? c0 : lc;
        ls = (rg == 2 * k + 1) ? s1 : ls;
        lc = (rg == 2 * k + 1) ? c1 : lc;
        s_base[k * 64 + lane] = (h ? s1 : s0) + inc[k] - v[k];
      }
      __builtin_amdgcn_wave_barrier();
    }
    u32 kb[2];
    u64 comp[2];
#pragma unroll
    for (int j = 0; j < 2; ++j) {
      kb[j] = 0u; comp[j] = 0ull;
      if (j == 0 ? nh > 0u : nh > (u32)NT) {
        kb[j] = s_base[key[j]];
        comp[j] = ((u64)hs[j] << 32) | he[j];
        if (act[j]) s_tmp[kb[j] + rk[j]] = comp[j];
      }
    }
    __syncthreads();
#pragma unroll
    for (int j = 0; j < 2; ++j) {
      if ((j == 0 || nh > (u32)NT) && act[j]) {
        const u32 n = s_cnt[key[j]];
        u32 p = kb[j];
        for (u32 k = 0; k < n; ++k) p += (s_tmp[kb[j] + k] < comp[j]) ? 1u : 0u;
        s_meta[p] = (hs[j] << 11) | (mp[j] >> 8);
        const float s0 = bflo(hsh[j].x), s1 = bfhi(hsh[j].x), s2 = bflo(hsh[j].y), s3 = bfhi(hsh[j].y);
        u32 at[8];
#pragma unroll
        for (int w = 0; w < 4; ++w) {
          float a[4];
#pragma unroll
          for (int u = 0; u < 4; ++u) {
            const f32x4 cv = *(const f32x4*)(s_cgt + w * 16 + u * 4);
            a[u] = cv.x * s0 + cv.y * s1 + cv.z * s2 + cv.w * s3;
          }
          at[2 * w] = cvtpk(a[0], a[1]);
          at[2 * w + 1] = cvtpk(a[2], a[3]);
        }
        u32x4* pa = (u32x4*)(smem + O_AT + p * 32);
        pa[0] = u32x4{at[0], at[1], at[2], at[3]};
        pa[1] = u32x4{at[4], at[5], at[6], at[7]};
        u32x4* pe = (u32x4*)(smem + O_EA + p * 32);
        pe[0] = hea[j][0];
        pe[1] = hea[j][1];
      }
    }
    if (hf == 0 && lc > 0) {
      const u32 p = ls + lc + lane;
      if (p < ls + ((lc + 7u) & ~7u)) {
        const u32 sl = (u32)(s_tmp[ls + lc - 1] >> 32);
        s_meta[p] = (sl << 11) | 15u;
        const u32x4 zz = {0u, 0u, 0u, 0u};
        u32x4* pe = (u32x4*)(smem + O_EA + p * 32);
        pe[0] = zz;
        pe[1] = zz;
      }
    }
    __syncthreads();

    f32x16 acc[4];
    if (first) {
#pragma unroll
      for (int r = 0; r < 4; ++r)
#pragma unroll
        for (int i = 0; i < 16; ++i) acc[r][i] = 0.f;
    } else {
      const f32x4* wsp = (const f32x4*)ws + ((size_t)blockIdx.x * NW + wv) * 1024 + lane;
#pragma unroll
      for (int r = 0; r < 4; ++r)
#pragma unroll
        for (int i = 0; i < 4; ++i) {
          const f32x4 t = wsp[(r * 4 + i) * 64];
          acc[r][4 * i] = t.x; acc[r][4 * i + 1] = t.y; acc[r][4 * i + 2] = t.z; acc[r][4 * i + 3] = t.w;
        }
    }
    const int ng = (int)((lc + 7u) >> 3);
    if (ng > 0) {
      MainCtx c;
      c.xb = x;
      c.xoff = hf * 256 + (lane & 31) * 8;
      c.myslot = (lane & 31) >> 2;
      c.atlane = O_AT + (lane & 3) * 8 + (ls + 4 * h) * 32;
      c.metaaddr = O_META + (ls + 4 * h) * 4;
      const bool match = (q4 >> 1) == ((c16 >> 2) & 1);
      c.eaaddr = match ? O_EA + (ls + 4 * (c16 >> 3) + (c16 & 3)) * 32 + 16 * (q4 & 1) : O_ZERO;
      c.eastride = match ? 256 : 0;
      c.wraddr = O_WR + hf * 4096 + lane * 16;
      u32x4 mc = *(const u32x4*)(smem + c.metaaddr);
      u32x4 eaf = *(const u32x4*)(smem + c.eaaddr);
      u32x2 xb0[8], xb1[8];
      load_block(c.xb, c.xoff, mc[0], mc[1], xb0);
      load_block(c.xb, c.xoff, mc[2], mc[3], xb1);
      int g = 0;
      for (; g < ng - 1; ++g) step<true>(smem, c, g, mc, eaf, xb0, xb1, acc);
      step<false>(smem, c, g, mc, eaf, xb0, xb1, acc);
    }
    if (end >= E) {

  u32x4 wf[8][2];
#pragma unroll
  for (int ks = 0; ks < 8; ++ks) { wf[ks][0] = u32x4{0u, 0u, 0u, 0u}; wf[ks][1] = u32x4{0u, 0u, 0u, 0u}; }
  if (wv < 8) {
    const int cb = 128 * (q4 & 1) + 64 * (q4 >> 1);
    const char* p = w_out + (size_t)cb * 512 + (wv * 32 + 2 * c16) * 2;
#pragma unroll
    for (int ks = 0; ks < 8; ++ks) {
      u32 L[8];
#pragma unroll
      for (int j = 0; j < 8; ++j) L[j] = *(const u32*)(p + (ks * 8 + j) * 512);
#pragma unroll
      for (int jj = 0; jj < 4; ++jj) {
        wf[ks][0][jj] = __builtin_amdgcn_perm(L[2 * jj + 1], L[2 * jj], 0x05040100u);
        wf[ks][1][jj] = __builtin_amdgcn_perm(L[2 * jj + 1], L[2 * jj], 0x07060302u);
      }
    }
  }
  {
    const u32 loc = s_inv[rg * 8 + ((lane & 31) >> 2)];
    if (loc != 0xffffffffu) {
      char* rowp = smem + O_TILE + (loc * 4 + (lane & 3)) * TROW + (hf * 128 + 16 * h) * 2;
#pragma unroll
      for (int qq = 0; qq < 4; ++qq) {
        u32 d[8];
#pragma unroll
        for (int a = 0; a < 4; ++a) {
          d[2 * a] = cvtpk(acc[0][4 * qq + a], acc[1][4 * qq + a]);
          d[2 * a + 1] = cvtpk(acc[2][4 * qq + a], acc[3][4 * qq + a]);
        }
        u32x4* pp = (u32x4*)(rowp + qq * 64);
        pp[0] = u32x4{d[0], d[1], d[2], d[3]};
        pp[1] = u32x4{d[4], d[5], d[6], d[7]};
      }
    }
  }
  __syncthreads();
  if (wv < 8) {
    const int cb = 128 * (q4 & 1) + 64 * (q4 >> 1);
    const char* tp = smem + O_TILE + c16 * TROW + cb * 2;
    char* op = out + ((size_t)n0 * 4 + c16) * 512 + (wv * 32 + 8 * q4) * 2;
    const u32 nrows = cntN * 4;
#pragma unroll 2
    for (int rb = 0; rb < MAXN * 4 / 16; ++rb) {
      f32x4 a0 = {0.f, 0.f, 0.f, 0.f}, a1 = {0.f, 0.f, 0.f, 0.f};
#pragma unroll
      for (int ks = 0; ks < 8; ++ks) {
        const u32x4 bf = *(const u32x4*)(tp + rb * 16 * TROW + ks * 16);
        a0 = MFMA16(__builtin_bit_cast(bf16x8, wf[ks][0]), __builtin_bit_cast(bf16x8, bf), a0);
        a1 = MFMA16(__builtin_bit_cast(bf16x8, wf[ks][1]), __builtin_bit_cast(bf16x8, bf), a1);
      }
      if ((u32)(rb * 16 + c16) < nrows) {
        const u32x4 o = {cvtpk(a0[0], a1[0]), cvtpk(a0[1], a1[1]), cvtpk(a0[2], a1[2]), cvtpk(a0[3], a1[3])};
        *(u32x4*)(op + (size_t)rb * 16 * 512) = o;
      }
    }
  }
  return;
    }
    int ln = tid0 & 63;
    asm volatile("" : "+v"(ln));
    f32x4* wsp = (f32x4*)ws + ((size_t)blockIdx.x * NW + wv) * 1024 + ln;
#pragma unroll
    for (int r = 0; r < 4; ++r)
#pragma unroll
      for (int i = 0; i < 4; ++i)
        wsp[(r * 4 + i) * 64] = f32x4{acc[r][4 * i], acc[r][4 * i + 1], acc[r][4 * i + 2], acc[r][4 * i + 3]};
    pos = end;
    first = false;
  }
}

}

namespace fast {
constexpr int ANT = 640;
constexpr int BNT = 512;
constexpr int SEGW = 128;
constexpr int LCAP = 8 * SEGW + 48;
constexpr int TROW = 528;
constexpr int MAXE = 256 * ANT;
constexpr size_t W_SLOT = 0;
constexpr size_t W_OVN = W_SLOT + 256 * 256 * 64;
constexpr size_t W_WR = W_OVN + 1024;
constexpr size_t W_WO = W_WR + 8192;
constexpr size_t W_OVF = W_WO + 131072;
constexpr size_t W_END = W_OVF + (size_t)MAXE * 8;
constexpr u32 INV = 0xffffffffu;
constexpr int O_CNT = 0;
constexpr int O_BASE = O_CNT + 1024;
constexpr int O_DEG = O_BASE + 1024;
constexpr int O_RANK = O_DEG + 256;
constexpr int O_INV = O_RANK + 256;
constexpr int O_ZERO = O_INV + 256;
constexpr int O_MISC = O_ZERO + 64;
constexpr int O_WRF = O_MISC + 64;
constexpr int O_EXP = O_WRF + 8192;
constexpr int O_TMP = O_EXP + 8 * SEGW * 8;
constexpr int O_META = O_TMP + LCAP * 4;
constexpr int O_AT = O_META + LCAP * 4;
constexpr int PCAP = 8 * SEGW;
constexpr int O_EA = O_AT + PCAP * 32;
constexpr int O_TILE = O_AT;
constexpr int LDS_BYTES = O_TILE + 160 * TROW;
static_assert(O_EA + PCAP * 32 + 32 <= LDS_BYTES, "LDS");
static_assert(LDS_BYTES <= 163840, "LDS");
static_assert((O_AT % 16) == 0 && (O_EA % 16) == 0 && (O_TILE % 16) == 0 && (O_META % 16) == 0, "align");

#define DPPADD(t, ctrl, rmask, bc) t += (u32)__builtin_amdgcn_update_dpp(0, (int)(t), ctrl, rmask, 0xf, bc)
DI u32 scan16(u32 t) {
  DPPADD(t, 0x111, 0xf, true);
  DPPADD(t, 0x112, 0xf, true);
  DPPADD(t, 0x114, 0xf, true);
  DPPADD(t, 0x118, 0xf, true);
  return t;
}
DI u32 scan32(u32 t) { t = scan16(t); DPPADD(t, 0x142, 0xa, false); return t; }
DI u32 scan64(u32 t) { t = scan32(t); DPPADD(t, 0x143, 0xc, false); return t; }
DI void wavesync() { __builtin_amdgcn_fence(__ATOMIC_RELEASE, "wavefront"); __builtin_amdgcn_wave_barrier(); __builtin_amdgcn_fence(__ATOMIC_ACQUIRE, "wavefront"); }

__global__ __launch_bounds__(ANT) void bin_kernel(
    const int* __restrict__ src, const int* __restrict__ dst, const unsigned short* __restrict__ w_r,
    const unsigned short* __restrict__ w_out, char* __restrict__ ws, int N, int E, int S, float invN) {
  __shared__ __attribute__((aligned(16))) u32 s_cnt[260];
  __shared__ __attribute__((aligned(16))) u32x4 s_img[1024];
  const int tid0 = threadIdx.x, tid = tid0, lane = tid & 63;
  const int wv = __builtin_amdgcn_readfirstlane(tid >> 6);
  const int i = blockIdx.x;
  ST(0);
  const int e = i * S + tid;
  const bool act = tid < S && e < E;
  u32 dn = 0, sn = 0;
  if (act) {
    dn = (u32)dst[e];
    sn = (u32)src[e];
  }
  if (tid < 260) s_cnt[tid] = 0u;
  s_img[tid] = u32x4{INV, INV, INV, INV};
  if (tid < 1024 - ANT) s_img[tid + ANT] = u32x4{INV, INV, INV, INV};
  if (wv == 9 && lane < 34) {
    const unsigned short* p;
    char* o;
    if (lane < 32) {
      const int id = i * 32 + lane;
      const int l2 = id & 63, h = (id >> 6) & 1, ks = (id >> 7) & 7, w8 = id >> 10;
      const int q4 = l2 >> 4, c16 = l2 & 15;
      p = w_out + (128 * (q4 & 1) + 64 * (q4 >> 1) + 8 * ks) * 256 + w8 * 32 + 2 * c16 + h;
      o = ws + W_WO + (size_t)id * 16;
    } else {
      const int id = i * 2 + (lane - 32);
      const int l2 = id & 63, r = id >> 6;
      const int kq = l2 >> 4, j = l2 & 15;
      p = w_r + (8 * (kq & 1)) * 256 + 8 * (16 * (kq >> 1) + j) + r;
      o = ws + W_WR + (size_t)id * 16;
    }
    u32 v[8];
#pragma unroll
    for (int k = 0; k < 8; ++k) v[k] = p[k * 256];
    *(u32x4*)o = u32x4{v[0] | (v[1] << 16), v[2] | (v[3] << 16), v[4] | (v[5] << 16), v[6] | (v[7] << 16)};
  }
  __syncthreads();
  u32 b = (u32)((float)(dn * 256u + 255u) * invN);
  b = umin(b, 255u);
  if ((((b + 1u) * (u32)N) >> 8) <= dn) b += 1u;
  if (((b * (u32)N) >> 8) > dn) b -= 1u;
  b = umin(b, 255u);
  const u32 loc = dn - ((b * (u32)N) >> 8);
  if (act) {
    const u32 rec = (sn << 16) | ((u32)tid << 6) | loc;
    const u32 rk = atomicAdd(&s_cnt[b], 1u);
    if (rk < 16u) {
      ((u32*)s_img)[b * 16u + rk] = rec;
    } else {
      const u32 o = atomicAdd(&s_cnt[256], 1u);
      ((u32x2*)(ws + W_OVF))[i * ANT + o] = u32x2{rec, b};
    }
  }
  __syncthreads();
  u32x4* po = (u32x4*)(ws + W_SLOT) + i * 4;
  po[(tid >> 2) * 1024 + (tid & 3)] = s_img[tid];
  if (tid < 1024 - ANT) po[((tid + ANT) >> 2) * 1024 + (tid & 3)] = s_img[tid + ANT];
  if (tid == ANT - 1) ((u32*)(ws + W_OVN))[i] = s_cnt[256];
  ST(1);
}

struct Ctx {
  const char* xb;
  u32 xoff;
  u32 js;
  u32 atl;
  u32 mad;
  u32 ead, east;
  u32 eao;
  u32 wra;
};

DI void load_x(const Ctx& c, u32x2 m, u32x4 (&X)[8]) {
#pragma unroll
  for (int i = 0; i < 2; ++i) {
    const char* p = c.xb + (size_t)(((m[i] >> 18) << 11) | c.xoff);
#pragma unroll
    for (int u = 0; u < 4; ++u) X[4 * i + u] = *(const u32x4*)(p + u * 512);
  }
}

DI f32x4 rwm(char* smem, const Ctx& c, const u32x4& eaf, int r) {
  const u32x4 w = *(const u32x4*)(smem + c.wra + r * 1024);
  const f32x4 z4 = {0.f, 0.f, 0.f, 0.f};
  return MFMA16(__builtin_bit_cast(bf16x8, eaf), __builtin_bit_cast(bf16x8, w), z4);
}

template <int PH>
DI void step(char* smem, const Ctx& c, int g, u32x2& m0, u32x2& m1, u32x4& eaf, f32x4 (&rw)[8],
             const u32x4 (&X)[8], u32x4 (&Y)[8], f32x16 (&acc)[8]) {
  u32x4 bt;
  u32 me = 0;
#pragma unroll
  for (int i = 0; i < 2; ++i) {
    const u32 a = ((m0[i] & 15u) == c.js) ? c.atl + (m0[i] & 0x7ff0u) * 2u : (u32)O_ZERO;
    const u32x2 t = *(const u32x2*)(smem + a);
    bt[2 * i] = t.x;
    bt[2 * i + 1] = t.y;
  }
  if (PH != 2) {
    asm volatile("" ::"v"(X[0]), "v"(X[1]), "v"(X[2]), "v"(X[3]), "v"(X[4]), "v"(X[5]), "v"(X[6]), "v"(X[7]));
    load_x(c, m1, Y);
    m0 = m1;
    m1 = *(const u32x2*)(smem + c.mad + (g + 2) * 16);
    __builtin_amdgcn_sched_barrier(0);
  }
#pragma unroll
  for (int r = 0; r < 8; ++r) {
    if (PH == 0 && r < 6) rw[r + 2] = rwm(smem, c, eaf, r + 2);
    if (PH == 1 && r == 2) me = *(const u32*)(smem + c.ead + ((g + 1) >> 1) * c.east);
    if (PH == 1 && r == 4) eaf = *(const u32x4*)(smem + c.eao + (me & 0x7ff0u) * 2u);
    u32x4 z;
#pragma unroll
    for (int i = 0; i < 2; ++i) {
      const float s = rw[r][(PH == 1 ? 2 : 0) + i];
#pragma unroll
      for (int up = 0; up < 2; ++up) {
        const u32 a = X[4 * i + 2 * up][r >> 1], b = X[4 * i + 2 * up + 1][r >> 1];
        z[2 * i + up] = (r & 1) ? cvtpk(bfhi(a) * s, bfhi(b) * s) : cvtpk(bflo(a) * s, bflo(b) * s);
      }
    }
    acc[r] = MFMA32(__builtin_bit_cast(bf16x8, z), __builtin_bit_cast(bf16x8, bt), acc[r]);
    if (PH == 1 && r >= 6) rw[r - 6] = rwm(smem, c, eaf, r - 6);
  }
}

#define STAMP_LDS 0
#define STA(k) do { } while (0)
#define STDUMP() do { } while (0)
__global__ __launch_bounds__(BNT, 2) void agg_kernel(const char* __restrict__ x, const char* __restrict__ sh,
                                                     const char* __restrict__ eattr, const u32* __restrict__ cg2,
                                                     char* __restrict__ out, char* __restrict__ ws, int N, int S,
                                                     float bscale) {
  __shared__ __attribute__((aligned(16))) char smem[LDS_BYTES + STAMP_LDS];
  const int tid0 = threadIdx.x;
  const int wv = __builtin_amdgcn_readfirstlane(tid0 >> 6);
  const int b = blockIdx.x;
  const u32 n0 = (u32)(b * N) >> 8;
  const u32 cntN = ((u32)((b + 1) * N) >> 8) - n0;
  u32* s_cnt = (u32*)(smem + O_CNT);
  u32* s_base = (u32*)(smem + O_BASE);
  u32* s_deg = (u32*)(smem + O_DEG);
  u32* s_rank = (u32*)(smem + O_RANK);
  u32* s_inv = (u32*)(smem + O_INV);
  u32* s_misc = (u32*)(smem + O_MISC);
  u32* s_meta = (u32*)(smem + O_META);
  u32x2* s_exp = (u32x2*)(smem + O_EXP) + wv * SEGW;
  u32* s_tmp = (u32*)(smem + O_TMP);
  STA(2);

  {
    if (tid0 >= 256 && tid0 < 256 + 64) s_deg[tid0 - 256] = 0u;
    if (tid0 >= 384 && tid0 < 384 + 64) s_inv[tid0 - 384] = 0xffffffffu;
    if (tid0 >= 448 && tid0 < 448 + 32) ((u32*)(smem + O_ZERO))[tid0 - 448] = (tid0 == 448 + 18) ? (u32)PCAP << 4 : 0u;
  }

  f32x16 acc[8];
#pragma unroll
  for (int r = 0; r < 8; ++r) {
#pragma unroll
    for (int k = 0; k < 16; ++k) {
      float z = 0.f;
      asm volatile("" : "+v"(z));
      acc[r][k] = z;
    }
  }

  int nseg = 1, nseg0 = 1, cps = 1;
  for (int seg = 0;; ++seg) {
    int tid = tid0;
    asm volatile("" : "+v"(tid));
    const int lane = tid & 63;
    const int q4 = lane >> 4, c16 = lane & 15;
    u32x4 cd[2];
    u32 eb;
    int sb = seg * SEGW;
    u32 ovn = 0;
    if (seg < nseg0) {
      const u32x4* sp = (const u32x4*)(ws + W_SLOT) + (b * 512 + tid) * 2;
      cd[0] = sp[0];
      cd[1] = sp[1];
      ovn = ((const u32*)(ws + W_OVN))[tid & 255];
      eb = (u32)((tid >> 1) * S);
    } else {
      const int idx = (seg - nseg0) * 8 + wv;
      const int sl = idx / cps, k0 = (idx - sl * cps) * SEGW + lane;
      const int n = (int)((const u32*)(ws + W_OVN))[sl];
      cd[0] = u32x4{INV, INV, INV, INV};
      cd[1] = cd[0];
#pragma unroll
      for (int j = 0; j < 2; ++j)
        if (k0 + j * 64 < n) {
          const u32x2 o = ((const u32x2*)(ws + W_OVF))[sl * ANT + k0 + j * 64];
          if (o.y == (u32)b) cd[0][j] = o.x;
        }
      eb = (u32)(sl * S);
      sb = 0;
    }
    const u32x4 wr0 = *(const u32x4*)(ws + W_WR + tid * 16);
    if (tid < 256) s_cnt[tid] = 0u;
    __syncthreads();
    u32 tc = 0;
#pragma unroll
    for (int k = 0; k < 8; ++k) tc += (cd[k >> 2][k & 3] != INV) ? 1u : 0u;
    const u32 tinc = scan64(tc);
    const int T = __builtin_amdgcn_readlane((int)tinc, 63);
    STA(3);
    {
      u32 p = tinc - tc - (u32)sb;
#pragma unroll
      for (int k = 0; k < 8; ++k) {
        const u32 r = cd[k >> 2][k & 3];
        const bool v = r != INV;
        if (k >= 4 && __builtin_amdgcn_ballot_w64(v) == 0ull) break;
        if (v && p < (u32)SEGW) s_exp[p] = u32x2{((r >> 16) << 18) | (eb + ((r >> 6) & 1023u)), r & 63u};
        p += v ? 1u : 0u;
      }
    }
    int Tn = T - sb;
    Tn = Tn < 0 ? 0 : (Tn > SEGW ? SEGW : Tn);
    wavesync();
    u32x2 mt[2], shv[2];
    u32x4 e0[2], e1[2];
    bool act[2];
    u32 eid[2];
#pragma unroll
    for (int j = 0; j < 2; ++j) {
      act[j] = j * 64 + lane < Tn;
      mt[j] = s_exp[j * 64 + lane];
      eid[j] = act[j] ? (mt[j].x & 0x3ffffu) : 0u;
    }
    STA(4);
    if (seg == 0) {
#pragma unroll
      for (int j = 0; j < 2; ++j)
        if (act[j]) atomicAdd(&s_deg[mt[j].y], 1u);
      if (lane == 0) atomicMax(&s_misc[0], (u32)T);
      if (ovn) atomicMax(&s_misc[1], ovn);
    }
    *(u32x4*)(smem + O_WRF + tid * 16) = wr0;
    if (tid < 2) *(u32x4*)(smem + O_EA + PCAP * 32 + tid * 16) = u32x4{0u, 0u, 0u, 0u};
#pragma unroll
    for (int j = 0; j < 2; ++j) shv[j] = *(const u32x2*)(sh + (size_t)eid[j] * 8);
    if (seg == 0) {
      STA(5);
      __syncthreads();
      const u32 d = umin(s_deg[lane], 127u);
      u32 gl = 0u, gh = 0u, el = 0xffffffffu, eh = 0xffu;
#pragma unroll
      for (int bit = 6; bit >= 0; --bit) {
        const bool on = (d >> bit) & 1u;
        const u64 B = __builtin_amdgcn_ballot_w64(on);
        const u32 bl = (u32)B, bh = (u32)(B >> 32);
        gl |= on ? 0u : (el & bl);
        gh |= on ? 0u : (eh & bh);
        el &= on ? bl : ~bl;
        eh &= on ? bh : ~bh;
      }
      const u32 rk = __builtin_popcount(gl) + __builtin_popcount(gh) +
                     __builtin_amdgcn_mbcnt_hi(eh, __builtin_amdgcn_mbcnt_lo(el, 0u));
      s_rank[lane] = rk;
      nseg0 = (int)((__builtin_amdgcn_readfirstlane(s_misc[0]) + SEGW - 1) / SEGW);
      nseg0 = nseg0 < 1 ? 1 : nseg0;
      cps = (int)((__builtin_amdgcn_readfirstlane(s_misc[1]) + SEGW - 1) / SEGW);
      nseg = nseg0 + 32 * cps;
      if (wv == 0 && (u32)lane < cntN) {
        const u32 slot = rk >> 3, r8 = rk & 7u;
        s_inv[((slot & 1u) ? 7u - r8 : r8) * 8u + slot] = (u32)lane;
      }
      wavesync();
    }
    STA(6);
    u32 key[2], rk[2], slot[2], comp[2];
#pragma unroll
    for (int j = 0; j < 2; ++j) {
      key[j] = 0u; rk[j] = 0u; slot[j] = 0u; comp[j] = 0u;
      if (j * 64 < Tn) {
        const u32 nr = s_rank[act[j] ? mt[j].y : 0u];
        slot[j] = nr >> 3;
        const u32 r8 = nr & 7u;
        const u32 bk = umin((u32)((float)(mt[j].x >> 18) * bscale), 31u);
        key[j] = ((slot[j] & 1u) ? 7u - r8 : r8) * 32u + bk;
        comp[j] = mt[j].x;
        if (act[j]) rk[j] = atomicAdd(&s_cnt[key[j]], 1u);
      }
    }
    e0[0] = *(const u32x4*)(eattr + (size_t)eid[0] * 32);
    e1[0] = *(const u32x4*)(eattr + (size_t)eid[0] * 32 + 16);
    __syncthreads();
    STA(7);
    u32 ls = 0, lc = 0;
    {
      const u32x4 c = *(const u32x4*)(s_cnt + 4 * lane);
      const u32 p1 = c.x, p2 = p1 + c.y, p3 = p2 + c.z, tot = p3 + c.w;
      const u32 inc = scan64(tot);
      u32 run = 0, prev = 0, adj = 0;
#pragma unroll
      for (int k = 0; k < 8; ++k) {
        const u32 ek = __builtin_amdgcn_readlane(inc, 8 * k + 7);
        const u32 ck = ek - prev;
        ls = (wv == k) ? run : ls;
        lc = (wv == k) ? ck : lc;
        adj = ((lane >> 3) == k) ? run - prev : adj;
        run += ck ? ((ck + 3u) & ~3u) : 4u;
        prev = ek;
      }
      const u32 ex = inc - tot + adj;
      *(u32x4*)(s_base + 4 * lane) = u32x4{ex, ex + p1, ex + p2, ex + p3};
      wavesync();
    }
    u32 kb[2], kn[2];
#pragma unroll
    for (int j = 0; j < 2; ++j) {
      kb[j] = 0u;
      kn[j] = 0u;
      if (j * 64 < Tn) {
        kb[j] = s_base[key[j]];
        kn[j] = act[j] ? s_cnt[key[j]] : 0u;
        if (act[j]) s_tmp[kb[j] + rk[j]] = comp[j];
      }
    }
    e0[1] = *(const u32x4*)(eattr + (size_t)eid[1] * 32);
    e1[1] = *(const u32x4*)(eattr + (size_t)eid[1] * 32 + 16);
    __syncthreads();
    STA(8);
    {
      u32 p[2] = {kb[0], kb[1]};
      const u32 nm = kn[0] > kn[1] ? kn[0] : kn[1];
#pragma clang loop unroll(disable) vectorize(disable) interleave(disable)
      for (u32 k = 0; k < nm; k += 4) {
        u32 t[2][4];
#pragma unroll
        for (int j = 0; j < 2; ++j)
#pragma unroll
          for (int q = 0; q < 4; ++q) t[j][q] = s_tmp[kb[j] + k + q];
#pragma unroll
        for (int j = 0; j < 2; ++j)
#pragma unroll
          for (int q = 0; q < 4; ++q) p[j] += (k + q < kn[j] && t[j][q] < comp[j]) ? 1u : 0u;
      }
#pragma unroll
      for (int j = 0; j < 2; ++j)
        if (act[j]) s_meta[p[j]] = (comp[j] & 0xfffc0000u) | ((u32)(wv * SEGW + j * 64 + lane) << 4) | slot[j];
    }
    const u32 lp = lc ? ((lc + 3u) & ~3u) : 4u;
    if (lane < 4 && lc + lane < lp) {
      const u32 p = ls + lc + lane;
      const u32 sl = lc ? (s_tmp[ls + lc - 1] & 0xfffc0000u) : 0u;
      s_meta[p] = sl | ((u32)PCAP << 4) | 15u;
    }
    __syncthreads();
    STA(9);

    u32x4 wf[8][2];
    {
      const int ng = (int)(lp >> 2);
      const int h = lane >> 5, j = lane & 31;
      Ctx c;
      c.xb = x;
      c.xoff = j * 16;
      c.js = j >> 2;
      c.atl = O_AT + (j & 3) * 8;
      c.mad = O_META + (ls + 2 * h) * 4;
      const bool match = (q4 >> 1) == ((c16 >> 2) & 1);
      c.ead = match ? O_META + (ls + 4 * ((c16 >> 1) & 1) + 2 * (c16 >> 3) + (c16 & 1)) * 4 : O_MISC + 8;
      c.east = match ? 32 : 0;
      c.eao = O_EA + 16 * (q4 & 1);
      c.wra = O_WRF + lane * 16;
      u32x2 m0 = *(const u32x2*)(smem + c.mad);
      u32x2 m1 = *(const u32x2*)(smem + c.mad + 16);
      u32x4 XA[8], XB[8];
      load_x(c, m0, XA);
      const u32 me0 = *(const u32*)(smem + c.ead);
      __builtin_amdgcn_sched_barrier(0);
      u32x4 at[2][2];
      u64 cga = (u64)cg2;
      asm volatile("" : "+s"(cga));
      const __attribute__((address_space(4))) u32* cgp = (const __attribute__((address_space(4))) u32*)cga;
#pragma unroll
      for (int j = 0; j < 2; ++j) {
        at[j][0] = u32x4{0u, 0u, 0u, 0u};
        at[j][1] = at[j][0];
        if (j * 64 < Tn) {
          const float s0 = bflo(shv[j].x), s1 = bfhi(shv[j].x), s2 = bflo(shv[j].y), s3 = bfhi(shv[j].y);
#pragma unroll
          for (int w = 0; w < 4; ++w) {
            float a[4];
#pragma unroll
            for (int u = 0; u < 4; ++u) {
              const u32 c0 = cgp[u * 8 + 0 + (w >> 1)], c1 = cgp[u * 8 + 2 + (w >> 1)];
              const u32 c2 = cgp[u * 8 + 4 + (w >> 1)], c3 = cgp[u * 8 + 6 + (w >> 1)];
              a[u] = ((w & 1) ? bfhi(c0) : bflo(c0)) * s0 + ((w & 1) ? bfhi(c1) : bflo(c1)) * s1 +
                     ((w & 1) ? bfhi(c2) : bflo(c2)) * s2 + ((w & 1) ? bfhi(c3) : bflo(c3)) * s3;
            }
            at[j][w >> 1][2 * (w & 1)] = cvtpk(a[0], a[1]);
            at[j][w >> 1][2 * (w & 1) + 1] = cvtpk(a[2], a[3]);
          }
        }
      }
#pragma unroll
      for (int j = 0; j < 2; ++j) {
        if (j * 64 < Tn) {
          const int p = wv * SEGW + j * 64 + lane;
          u32x4* pa = (u32x4*)(smem + O_AT + p * 32);
          u32x4* pe = (u32x4*)(smem + O_EA + p * 32);
          pa[0] = at[j][0];
          pa[1] = at[j][1];
          pe[0] = e0[j];
          pe[1] = e1[j];
        }
      }
      __syncthreads();
      f32x4 rw[8];
      u32x4 eaf = *(const u32x4*)(smem + c.eao + (me0 & 0x7ff0u) * 2u);
#pragma unroll
      for (int r = 2; r < 8; ++r) rw[r] = f32x4{0.f, 0.f, 0.f, 0.f};
      rw[0] = rwm(smem, c, eaf, 0);
      rw[1] = rwm(smem, c, eaf, 1);
      int g = 0;
      for (; g + 2 < ng; g += 2) {
        step<0>(smem, c, g, m0, m1, eaf, rw, XA, XB, acc);
        step<1>(smem, c, g + 1, m0, m1, eaf, rw, XB, XA, acc);
      }
      if (g + 1 < ng) {
        step<0>(smem, c, g, m0, m1, eaf, rw, XA, XB, acc);
        ++g;
#pragma unroll
        for (int k = 0; k < 8; ++k) XA[k] = XB[k];
#pragma unroll
        for (int r = 0; r < 8; ++r) { rw[r][0] = rw[r][2]; rw[r][1] = rw[r][3]; }
      } else {
#pragma unroll
        for (int r = 2; r < 8; ++r) rw[r] = rwm(smem, c, eaf, r);
      }
      step<2>(smem, c, g, m0, m1, eaf, rw, XA, XB, acc);
      __builtin_amdgcn_sched_barrier(0);
      {
        const char* wp = ws + W_WO + (size_t)(wv * 16 * 64 + lane) * 16;
#pragma unroll
        for (int ks = 0; ks < 8; ++ks) {
          wf[ks][0] = *(const u32x4*)(wp + (ks * 2 + 0) * 1024);
          wf[ks][1] = *(const u32x4*)(wp + (ks * 2 + 1) * 1024);
        }
        __builtin_amdgcn_sched_barrier(0);
      }
    }
    STA(10);
    __syncthreads();
    if (seg + 1 < nseg) continue;

    {
      const u32 loc = s_inv[wv * 8 + ((lane & 31) >> 2)];
      if (loc != 0xffffffffu) {
        char* rowp = smem + O_TILE + (loc * 4 + (lane & 3)) * TROW + (lane >> 5) * 64;
#pragma unroll
        for (int a = 0; a < 16; ++a)
          *(u32x4*)(rowp + (a >> 2) * 128 + (a & 3) * 16) =
              u32x4{cvtpk(acc[0][a], acc[1][a]), cvtpk(acc[2][a], acc[3][a]), cvtpk(acc[4][a], acc[5][a]),
                    cvtpk(acc[6][a], acc[7][a])};
      }
    }
    STA(11);
    __syncthreads();
    STA(12);
    {
      const int cb = 128 * (q4 & 1) + 64 * (q4 >> 1);
      const char* tp = smem + O_TILE + c16 * TROW + cb * 2;
      char* op = out + ((size_t)n0 * 4 + c16) * 512 + (wv * 32 + 8 * q4) * 2;
      const u32 nrows = cntN * 4;
#pragma unroll 2
      for (int rb = 0; rb < 10; ++rb) {
        f32x4 a0 = {0.f, 0.f, 0.f, 0.f}, a1 = {0.f, 0.f, 0.f, 0.f};
#pragma unroll
        for (int ks = 0; ks < 8; ++ks) {
          const u32x4 bf = *(const u32x4*)(tp + rb * 16 * TROW + ks * 16);
          a0 = MFMA16(__builtin_bit_cast(bf16x8, wf[ks][0]), __builtin_bit_cast(bf16x8, bf), a0);
          a1 = MFMA16(__builtin_bit_cast(bf16x8, wf[ks][1]), __builtin_bit_cast(bf16x8, bf), a1);
        }
        if ((u32)(rb * 16 + c16) < nrows) {
          const u32x4 o = {cvtpk(a0[0], a1[0]), cvtpk(a0[1], a1[1]), cvtpk(a0[2], a1[2]), cvtpk(a0[3], a1[3])};
          *(u32x4*)(op + (size_t)rb * 16 * 512) = o;
        }
      }
    }
    STA(13);
    STDUMP();
    return;
  }
}
}

extern "C" void kernel_launch(void* const* d_in, const int* in_sizes, int n_in, void* d_out, int out_size,
                              void* d_ws, size_t ws_size, hipStream_t stream) {
  const int N = in_sizes[0] / 1024;
  const int E = in_sizes[1];
  if (N <= 0) return;
  if (N <= 10240 && E >= 1 && E <= fast::MAXE && ws_size >= fast::W_END) {
    int S = (E + 255) / 256;
    if (S < 1) S = 1;
    fast::bin_kernel<<<256, fast::ANT, 0, stream>>>((const int*)d_in[1], (const int*)d_in[2],
                                                    (const unsigned short*)d_in[6], (const unsigned short*)d_in[7],
                                                    (char*)d_ws, N, E, S, 1.0f / (float)N);
    fast::agg_kernel<<<256, fast::BNT, 0, stream>>>((const char*)d_in[0], (const char*)d_in[3], (const char*)d_in[4],
                                                    (const u32*)d_in[5], (char*)d_out, (char*)d_ws, N, S,
                                                    32.0f / (float)N);
    return;
  }
  int G = (N + gen::MAXN - 1) / gen::MAXN;
  if (G < 256) G = 256;
  if (G > N) G = N;
  size_t gmax = ws_size / ((size_t)gen::NW * 16384);
  if (gmax > 65536) gmax = 65536;
  if (gmax < 1) return;
  for (int b0 = 0; b0 < G; b0 += (int)gmax) {
    const int g = (G - b0 < (int)gmax) ? G - b0 : (int)gmax;
    gen::mace_kernel<<<g, gen::NT, 0, stream>>>(
        (const char*)d_in[0], (const int*)d_in[1], (const int*)d_in[2], (const char*)d_in[3], (const char*)d_in[4],
        (const unsigned short*)d_in[5], (const char*)d_in[6], (const char*)d_in[7], (char*)d_out, (float*)d_ws, N, E,
        b0, G, (float)gen::NB / (float)N);
  }
}
```

```cpp
#include <hip/hip_runtime.h>
#include <hip/hip_bf16.h>
#include <stdint.h>

typedef unsigned int u32;
typedef unsigned long long u64;
typedef __attribute__((ext_vector_type(8))) short bf16x8;
typedef __attribute__((ext_vector_type(2))) __bf16 bf16x2_t;
typedef __attribute__((ext_vector_type(2))) float f32x2;
typedef __attribute__((ext_vector_type(4))) float f32x4;
typedef __attribute__((ext_vector_type(16))) float f32x16;
typedef __attribute__((ext_vector_type(2))) unsigned int u32x2;
typedef __attribute__((ext_vector_type(4))) unsigned int u32x4;

#define DI __device__ __forceinline__
#define ST(k) do { } while (0)
#define MFMA32(a, b, c) __builtin_amdgcn_mfma_f32_32x32x16_bf16((a), (b), (c), 0, 0, 0)
#define MFMA16(a, b, c) __builtin_amdgcn_mfma_f32_16x16x32_bf16((a), (b), (c), 0, 0, 0)

DI u32 cvtpk(float a, float b) {
  f32x2 v = {a, b};
  bf16x2_t r = __builtin_convertvector(v, bf16x2_t);
  return __builtin_bit_cast(u32, r);
}
DI float bflo(u32 v) { return __builtin_bit_cast(float, v << 16); }
DI float bfhi(u32 v) { return __builtin_bit_cast(float, v & 0xffff0000u); }
DI u32 mbcnt(u64 m) { return __builtin_amdgcn_mbcnt_hi((u32)(m >> 32), __builtin_amdgcn_mbcnt_lo((u32)m, 0u)); }
DI u32 umin(u32 a, u32 b) { return a < b ? a : b; }

namespace gen {
constexpr int NT = 768, NW = 12, NRG = 6, NB = 32;
constexpr int MAXN = 40;
constexpr int CAP = 944;
constexpr int LCAP = CAP + 48;
constexpr int CAPW = 128;
constexpr int WMIN = 512;
constexpr int SU = 9;
constexpr int TROW = 528;
constexpr int PFROWS = 1536;

constexpr int O_CNT = 0;
constexpr int O_BASE = O_CNT + 192 * 4;
constexpr int O_DEG = O_BASE + 192 * 4;
constexpr int O_MAP = O_DEG + 64 * 4;
constexpr int O_INV = O_MAP + 64 * 4;
constexpr int O_CGT = O_INV + 64 * 4;
constexpr int O_ZERO = O_CGT + 64 * 4;
constexpr int O_MISC = O_ZERO + 64;
constexpr int O_WR = O_MISC + 64;
constexpr int O_META = O_WR + 8192;
constexpr int O_AT = O_META + LCAP * 4;
constexpr int O_EA = O_AT + LCAP * 32;
constexpr int O_TILE = O_EA + LCAP * 32;
constexpr int O_CANDQ = O_TILE;
constexpr int O_CANDI = O_CANDQ + NW * CAPW * 16;
constexpr int O_HITE = O_CANDI + NW * CAPW * 4;
constexpr int O_HITL = O_HITE + 1024 * 4;
constexpr int O_TMP = O_HITL + 1024 * 4;
constexpr int LDS_BYTES = O_TILE + MAXN * 4 * TROW;
static_assert(O_TMP + LCAP * 8 <= LDS_BYTES, "alias region too small");
static_assert(LDS_BYTES <= 163840, "LDS");
static_assert((O_AT % 16) == 0 && (O_EA % 16) == 0 && (O_TILE % 16) == 0 && (O_WR % 16) == 0, "align");

struct MainCtx {
  const char* xb;
  u32 xoff;
  u32 myslot;
  u32 atlane;
  u32 metaaddr;
  u32 eaaddr, eastride;
  u32 wraddr;
};

DI void load_block(const char* xb, u32 xoff, u32 m0, u32 m1, u32x2 (&buf)[8]) {
  const u32 o0 = (m0 & 0xfffff800u) | xoff, o1 = (m1 & 0xfffff800u) | xoff;
  const char* p0 = xb + (size_t)o0;
  const char* p1 = xb + (size_t)o1;
#pragma unroll
  for (int u = 0; u < 4; ++u) buf[u] = *(const u32x2*)(p0 + u * 512);
#pragma unroll
  for (int u = 0; u < 4; ++u) buf[4 + u] = *(const u32x2*)(p1 + u * 512);
}

DI u32x4 make_z(const u32x2 (&buf)[8], int r, float s0, float s1) {
  u32x4 z;
#pragma unroll
  for (int i = 0; i < 2; ++i) {
    const float s = i ? s1 : s0;
#pragma unroll
    for (int up = 0; up < 2; ++up) {
      const u32 a = (r & 2) ? buf[4 * i + 2 * up].y : buf[4 * i + 2 * up].x;
      const u32 b = (r & 2) ? buf[4 * i + 2 * up + 1].y : buf[4 * i + 2 * up + 1].x;
      z[2 * i + up] = (r & 1) ? cvtpk(bfhi(a) * s, bfhi(b) * s) : cvtpk(bflo(a) * s, bflo(b) * s);
    }
  }
  return z;
}

template <bool PF>
DI void step(char* smem, const MainCtx& c, int g, u32x4& mc, u32x4& eaf, u32x2 (&xb0)[8], u32x2 (&xb1)[8],
             f32x16 (&acc)[4]) {
  const u32 ab = c.atlane + g * 256;
  u32x2 bt[4];
#pragma unroll
  for (int k = 0; k < 4; ++k) {
    const u32 a = ((mc[k] & 15u) == c.myslot) ? ab + k * 32 : (u32)O_ZERO;
    bt[k] = *(const u32x2*)(smem + a);
  }
  if (PF) mc = *(const u32x4*)(smem + c.metaaddr + (g + 1) * 32);
  f32x4 rw[4];
  const f32x4 z4 = {0.f, 0.f, 0.f, 0.f};
  u32 wra = c.wraddr;
  asm volatile("" : "+v"(wra));
#pragma unroll
  for (int r = 0; r < 4; ++r) {
    const u32x4 wr = *(const u32x4*)(smem + wra + r * 1024);
    rw[r] = MFMA16(__builtin_bit_cast(bf16x8, eaf), __builtin_bit_cast(bf16x8, wr), z4);
  }
  if (PF) eaf = *(const u32x4*)(smem + c.eaaddr + (g + 1) * c.eastride);
  {
    const u32x4 b0 = {bt[0].x, bt[0].y, bt[1].x, bt[1].y};
#pragma unroll
    for (int r = 0; r < 4; ++r) {
      const u32x4 z = make_z(xb0, r, rw[r][0], rw[r][1]);
      acc[r] = MFMA32(__builtin_bit_cast(bf16x8, z), __builtin_bit_cast(bf16x8, b0), acc[r]);
    }
    if (PF) load_block(c.xb, c.xoff, mc[0], mc[1], xb0);
  }
  {
    const u32x4 b1 = {bt[2].x, bt[2].y, bt[3].x, bt[3].y};
#pragma unroll
    for (int r = 0; r < 4; ++r) {
      const u32x4 z = make_z(xb1, r, rw[r][2], rw[r][3]);
      acc[r] = MFMA32(__builtin_bit_cast(bf16x8, z), __builtin_bit_cast(bf16x8, b1), acc[r]);
    }
    if (PF) load_block(c.xb, c.xoff, mc[2], mc[3], xb1);
  }
}

__global__ __launch_bounds__(NT, 3) void mace_kernel(
    const char* __restrict__ x, const int* __restrict__ src, const int* __restrict__ dst,
    const char* __restrict__ sh, const char* __restrict__ eattr, const unsigned short* __restrict__ cg,
    const char* __restrict__ w_r, const char* __restrict__ w_out, char* __restrict__ out,
    float* __restrict__ ws, int N, int E, int b0, int G, float bscale) {
  __shared__ __attribute__((aligned(16))) char smem[LDS_BYTES];
  const int tid0 = threadIdx.x;
  const int wv = __builtin_amdgcn_readfirstlane(tid0 >> 6);
  const int rg = wv >> 1, hf = wv & 1;
  const int b = b0 + blockIdx.x;
  const u32 n0 = (u32)(((u64)b * (u64)N) / (u64)G);
  const u32 cntN = (u32)((((u64)b + 1) * (u64)N) / (u64)G) - n0;

  u32* s_cnt = (u32*)(smem + O_CNT);
  u32* s_base = (u32*)(smem + O_BASE);
  u32* s_deg = (u32*)(smem + O_DEG);
  u32* s_map = (u32*)(smem + O_MAP);
  u32* s_inv = (u32*)(smem + O_INV);
  float* s_cgt = (float*)(smem + O_CGT);
  u32* s_misc = (u32*)(smem + O_MISC);
  u32* s_hitE = (u32*)(smem + O_HITE);
  u32* s_hitL = (u32*)(smem + O_HITL);
  u64* s_tmp = (u64*)(smem + O_TMP);
  u32* s_meta = (u32*)(smem + O_META);

  if (wv < 2) {
    const int lane = tid0 & 63;
    const int q4 = lane >> 4, c16 = lane & 15;
    u32x4 wr[4];
    u32x2 L[8];
    const char* p = w_r + (size_t)(8 * (q4 & 1)) * 512 + (hf * 128 + 64 * (q4 >> 1) + 4 * c16) * 2;
#pragma unroll
    for (int j = 0; j < 8; ++j) L[j] = *(const u32x2*)(p + j * 512);
#pragma unroll
    for (int jj = 0; jj < 4; ++jj) {
      wr[0][jj] = __builtin_amdgcn_perm(L[2 * jj + 1].x, L[2 * jj].x, 0x05040100u);
      wr[1][jj] = __builtin_amdgcn_perm(L[2 * jj + 1].x, L[2 * jj].x, 0x07060302u);
      wr[2][jj] = __builtin_amdgcn_perm(L[2 * jj + 1].y, L[2 * jj].y, 0x05040100u);
      wr[3][jj] = __builtin_amdgcn_perm(L[2 * jj + 1].y, L[2 * jj].y, 0x07060302u);
    }
#pragma unroll
    for (int r = 0; r < 4; ++r) *(u32x4*)(smem + O_WR + hf * 4096 + r * 1024 + lane * 16) = wr[r];
  }
  if (wv == 2) {
    const int tid = tid0 & 63;
    const int w = tid >> 4, u = (tid >> 2) & 3, v = tid & 3;
    s_cgt[tid] = bflo((u32)cg[u * 16 + v * 4 + w]);
    s_map[tid] = (u32)(tid % NRG) | ((u32)(tid / NRG) << 8);
    const u32 l = (u32)((tid & 7) * NRG + (tid >> 3));
    s_inv[tid] = (tid < NRG * 8 && l < cntN) ? l : 0xffffffffu;
    if (tid < 16) ((u32*)(smem + O_ZERO))[tid] = 0u;
  }

  bool first = true, again = false;
  int pos = 0;
  int W = (E + 3) & ~3;
  for (;;) {
    if (again) __syncthreads();
    again = true;
    int tid = tid0;
    asm volatile("" : "+v"(tid));
    const int lane = tid & 63;
    const int q4 = lane >> 4, c16 = lane & 15, h = lane >> 5;
    u32 pfv = 0;
    {
      const u32 line = (u32)(b >> 3) * NT + tid;
      if (first && line < (u32)PFROWS * 16u && line < (u32)N * 16u) pfv = *(const u32*)(x + (size_t)line * 128);
    }
    if (tid < 192) s_cnt[tid] = 0u;
    if (tid >= 192 && tid < 256) s_deg[tid - 192] = 0u;
    if (tid >= 256 && tid < 258) s_misc[tid - 256] = 0u;
    __syncthreads();
    const int end = (E - pos < W) ? E : pos + W;

    {
      const u32x4* dq = (const u32x4*)dst;
      u32x4* cq = (u32x4*)(smem + O_CANDQ) + wv * CAPW;
      u32* ci = (u32*)(smem + O_CANDI) + wv * CAPW;
      const int q0 = pos >> 2, qf = end >> 2;
      u32 nc = 0;
      for (int qb = q0 + wv * 64; qb < qf; qb += NT * SU) {
        u32x4 v[SU];
#pragma unroll
        for (int k = 0; k < SU; ++k) {
          const int q = qb + k * NT + lane;
          v[k] = dq[q < qf ? q : qf - 1];
        }
#pragma unroll
        for (int k = 0; k < SU; ++k) {
          const int q = qb + k * NT + lane;
          const u32 m = umin(umin(v[k].x - n0, v[k].y - n0), umin(v[k].z - n0, v[k].w - n0));
          const bool hit = (m < cntN) && (q < qf);
          const u64 mask = __ballot(hit);
          if (mask) {
            const u32 p = nc + mbcnt(mask);
            if (hit && p < (u32)CAPW) { cq[p] = v[k]; ci[p] = (u32)q; }
            nc += (u32)__popcll(mask);
          }
        }
      }
      if (wv == NW - 1 && (end & 3)) {
        u32x4 v;
        const int e4 = qf * 4;
        v.x = (u32)dst[e4];
        v.y = (e4 + 1 < end) ? (u32)dst[e4 + 1] : n0 - 1u;
        v.z = (e4 + 2 < end) ? (u32)dst[e4 + 2] : n0 - 1u;
        v.w = n0 - 1u;
        if (lane == 0 && nc < (u32)CAPW) { cq[nc] = v; ci[nc] = (u32)qf; }
        nc += 1;
      }
      if (nc > (u32)CAPW) { if (lane == 0) s_misc[1] = 1u; nc = CAPW; }
      __builtin_amdgcn_wave_barrier();
      for (u32 i0 = 0; i0 < nc; i0 += 64) {
        const u32 i = i0 + lane;
        const bool valid = i < nc;
        const u32x4 v = cq[valid ? i : 0];
        const u32 q = ci[valid ? i : 0];
        const u32 t[4] = {v.x - n0, v.y - n0, v.z - n0, v.w - n0};
        u64 mk[4];
        u32 tot = 0;
#pragma unroll
        for (int k = 0; k < 4; ++k) { mk[k] = __ballot(valid && t[k] < cntN); tot += (u32)__popcll(mk[k]); }
        u32 bs = 0;
        if (lane == 0) bs = atomicAdd(&s_misc[0], tot);
        bs = __builtin_amdgcn_readfirstlane(bs);
#pragma unroll
        for (int k = 0; k < 4; ++k) {
          const u32 p = bs + mbcnt(mk[k]);
          if (valid && t[k] < cntN && p < (u32)CAP) {
            s_hitE[p] = 4 * q + k;
            s_hitL[p] = t[k];
            atomicAdd(&s_deg[t[k]], 1u);
          }
          bs += (u32)__popcll(mk[k]);
        }
      }
    }
    asm volatile("" ::"v"(pfv));
    __syncthreads();
    const u32 nh = __builtin_amdgcn_readfirstlane(s_misc[0]);
    if (nh > (u32)CAP || __builtin_amdgcn_readfirstlane(s_misc[1]) != 0u) {
      W = ((W >> 1) + 3) & ~3;
      if (W < WMIN) W = WMIN;
      continue;
    }

    u32 he[2], hl[2], hs[2];
    u32x2 hsh[2];
    u32x4 hea[2][2];
    bool act[2];
#pragma unroll
    for (int j = 0; j < 2; ++j) {
      const u32 i = tid + j * NT;
      act[j] = i < nh;
      he[j] = 0u; hl[j] = 0u; hs[j] = 0u;
      hsh[j] = u32x2{0u, 0u};
      hea[j][0] = u32x4{0u, 0u, 0u, 0u};
      hea[j][1] = u32x4{0u, 0u, 0u, 0u};
      if (j == 0 ? nh > 0u : nh > (u32)NT) {
        const u32 ii = act[j] ? i : 0;
        he[j] = s_hitE[ii];
        hl[j] = s_hitL[ii];
        hs[j] = (u32)src[he[j]];
        hsh[j] = *(const u32x2*)(sh + (size_t)he[j] * 8);
        hea[j][0] = *(const u32x4*)(eattr + (size_t)he[j] * 32);
        hea[j][1] = *(const u32x4*)(eattr + (size_t)he[j] * 32 + 16);
      }
    }
    if (first && wv == NW - 1) {
      const u32 d = s_deg[lane];
      u32 rank = 0;
      for (u32 j = 0; j < cntN; ++j) {
        const u32 dj = __builtin_amdgcn_readlane(d, j);
        rank += (dj > d || (dj == d && j < (u32)lane)) ? 1u : 0u;
      }
      const u32 slot = rank / NRG, r = rank % NRG;
      const u32 g = (slot & 1) ? (NRG - 1 - r) : r;
      s_inv[lane] = 0xffffffffu;
      __builtin_amdgcn_wave_barrier();
      if ((u32)lane < cntN) { s_map[lane] = g | (slot << 8); s_inv[g * 8 + slot] = lane; }
    }
    __syncthreads();

    u32 key[2], rk[2], mp[2];
#pragma unroll
    for (int j = 0; j < 2; ++j) {
      key[j] = 0u; rk[j] = 0u; mp[j] = 0u;
      if (j == 0 ? nh > 0u : nh > (u32)NT) {
        mp[j] = s_map[hl[j]];
        u32 bk = (u32)((float)hs[j] * bscale);
        bk = umin(bk, NB - 1);
        key[j] = (mp[j] & 255u) * NB + bk;
        if (act[j]) rk[j] = atomicAdd(&s_cnt[key[j]], 1u);
      }
    }
    __syncthreads();
    u32 ls = 0, lc = 0;
    {
      u32 v[3], inc[3];
#pragma unroll
      for (int k = 0; k < 3; ++k) {
        v[k] = s_cnt[k * 64 + lane];
        u32 t = v[k];
        t += (u32)__builtin_amdgcn_update_dpp(0, (int)t, 0x111, 0xf, 0xf, true);
        t += (u32)__builtin_amdgcn_update_dpp(0, (int)t, 0x112, 0xf, 0xf, true);
        t += (u32)__builtin_amdgcn_update_dpp(0, (int)t, 0x114, 0xf, 0xf, true);
        t += (u32)__builtin_amdgcn_update_dpp(0, (int)t, 0x118, 0xf, 0xf, true);
        t += (u32)__builtin_amdgcn_update_dpp(0, (int)t, 0x142, 0xa, 0xf, false);
        inc[k] = t;
      }
      u32 run = 0;
#pragma unroll
      for (int k = 0; k < 3; ++k) {
        const u32 c0 = __builtin_amdgcn_readlane(inc[k], 31);
        const u32 c1 = __builtin_amdgcn_readlane(inc[k], 63);
        const u32 s0 = run, s1 = run + ((c0 + 7u) & ~7u);
        run = s1 + ((c1 + 7u) & ~7u);
        ls = (rg == 2 * k) ? s0 : ls;
        lc = (rg == 2 * k) ? c0 : lc;
        ls = (rg == 2 * k + 1) ? s1 : ls;
        lc = (rg == 2 * k + 1) ? c1 : lc;
        s_base[k * 64 + lane] = (h ? s1 : s0) + inc[k] - v[k];
      }
      __builtin_amdgcn_wave_barrier();
    }
    u32 kb[2];
    u64 comp[2];
#pragma unroll
    for (int j = 0; j < 2; ++j) {
      kb[j] = 0u; comp[j] = 0ull;
      if (j == 0 ? nh > 0u : nh > (u32)NT) {
        kb[j] = s_base[key[j]];
        comp[j] = ((u64)hs[j] << 32) | he[j];
        if (act[j]) s_tmp[kb[j] + rk[j]] = comp[j];
      }
    }
    __syncthreads();
#pragma unroll
    for (int j = 0; j < 2; ++j) {
      if ((j == 0 || nh > (u32)NT) && act[j]) {
        const u32 n = s_cnt[key[j]];
        u32 p = kb[j];
        for (u32 k = 0; k < n; ++k) p += (s_tmp[kb[j] + k] < comp[j]) ? 1u : 0u;
        s_meta[p] = (hs[j] << 11) | (mp[j] >> 8);
        const float s0 = bflo(hsh[j].x), s1 = bfhi(hsh[j].x), s2 = bflo(hsh[j].y), s3 = bfhi(hsh[j].y);
        u32 at[8];
#pragma unroll
        for (int w = 0; w < 4; ++w) {
          float a[4];
#pragma unroll
          for (int u = 0; u < 4; ++u) {
            const f32x4 cv = *(const f32x4*)(s_cgt + w * 16 + u * 4);
            a[u] = cv.x * s0 + cv.y * s1 + cv.z * s2 + cv.w * s3;
          }
          at[2 * w] = cvtpk(a[0], a[1]);
          at[2 * w + 1] = cvtpk(a[2], a[3]);
        }
        u32x4* pa = (u32x4*)(smem + O_AT + p * 32);
        pa[0] = u32x4{at[0], at[1], at[2], at[3]};
        pa[1] = u32x4{at[4], at[5], at[6], at[7]};
        u32x4* pe = (u32x4*)(smem + O_EA + p * 32);
        pe[0] = hea[j][0];
        pe[1] = hea[j][1];
      }
    }
    if (hf == 0 && lc > 0) {
      const u32 p = ls + lc + lane;
      if (p < ls + ((lc + 7u) & ~7u)) {
        const u32 sl = (u32)(s_tmp[ls + lc - 1] >> 32);
        s_meta[p] = (sl << 11) | 15u;
        const u32x4 zz = {0u, 0u, 0u, 0u};
        u32x4* pe = (u32x4*)(smem + O_EA + p * 32);
        pe[0] = zz;
        pe[1] = zz;
      }
    }
    __syncthreads();

    f32x16 acc[4];
    if (first) {
#pragma unroll
      for (int r = 0; r < 4; ++r)
#pragma unroll
        for (int i = 0; i < 16; ++i) acc[r][i] = 0.f;
    } else {
      const f32x4* wsp = (const f32x4*)ws + ((size_t)blockIdx.x * NW + wv) * 1024 + lane;
#pragma unroll
      for (int r = 0; r < 4; ++r)
#pragma unroll
        for (int i = 0; i < 4; ++i) {
          const f32x4 t = wsp[(r * 4 + i) * 64];
          acc[r][4 * i] = t.x; acc[r][4 * i + 1] = t.y; acc[r][4 * i + 2] = t.z; acc[r][4 * i + 3] = t.w;
        }
    }
    const int ng = (int)((lc + 7u) >> 3);
    if (ng > 0) {
      MainCtx c;
      c.xb = x;
      c.xoff = hf * 256 + (lane & 31) * 8;
      c.myslot = (lane & 31) >> 2;
      c.atlane = O_AT + (lane & 3) * 8 + (ls + 4 * h) * 32;
      c.metaaddr = O_META + (ls + 4 * h) * 4;
      const bool match = (q4 >> 1) == ((c16 >> 2) & 1);
      c.eaaddr = match ? O_EA + (ls + 4 * (c16 >> 3) + (c16 & 3)) * 32 + 16 * (q4 & 1) : O_ZERO;
      c.eastride = match ? 256 : 0;
      c.wraddr = O_WR + hf * 4096 + lane * 16;
      u32x4 mc = *(const u32x4*)(smem + c.metaaddr);
      u32x4 eaf = *(const u32x4*)(smem + c.eaaddr);
      u32x2 xb0[8], xb1[8];
      load_block(c.xb, c.xoff, mc[0], mc[1], xb0);
      load_block(c.xb, c.xoff, mc[2], mc[3], xb1);
      int g = 0;
      for (; g < ng - 1; ++g) step<true>(smem, c, g, mc, eaf, xb0, xb1, acc);
      step<false>(smem, c, g, mc, eaf, xb0, xb1, acc);
    }
    if (end >= E) {

  u32x4 wf[8][2];
#pragma unroll
  for (int ks = 0; ks < 8; ++ks) { wf[ks][0] = u32x4{0u, 0u, 0u, 0u}; wf[ks][1] = u32x4{0u, 0u, 0u, 0u}; }
  if (wv < 8) {
    const int cb = 128 * (q4 & 1) + 64 * (q4 >> 1);
    const char* p = w_out + (size_t)cb * 512 + (wv * 32 + 2 * c16) * 2;
#pragma unroll
    for (int ks = 0; ks < 8; ++ks) {
      u32 L[8];
#pragma unroll
      for (int j = 0; j < 8; ++j) L[j] = *(const u32*)(p + (ks * 8 + j) * 512);
#pragma unroll
      for (int jj = 0; jj < 4; ++jj) {
        wf[ks][0][jj] = __builtin_amdgcn_perm(L[2 * jj + 1], L[2 * jj], 0x05040100u);
        wf[ks][1][jj] = __builtin_amdgcn_perm(L[2 * jj + 1], L[2 * jj], 0x07060302u);
      }
    }
  }
  {
    const u32 loc = s_inv[rg * 8 + ((lane & 31) >> 2)];
    if (loc != 0xffffffffu) {
      char* rowp = smem + O_TILE + (loc * 4 + (lane & 3)) * TROW + (hf * 128 + 16 * h) * 2;
#pragma unroll
      for (int qq = 0; qq < 4; ++qq) {
        u32 d[8];
#pragma unroll
        for (int a = 0; a < 4; ++a) {
          d[2 * a] = cvtpk(acc[0][4 * qq + a], acc[1][4 * qq + a]);
          d[2 * a + 1] = cvtpk(acc[2][4 * qq + a], acc[3][4 * qq + a]);
        }
        u32x4* pp = (u32x4*)(rowp + qq * 64);
        pp[0] = u32x4{d[0], d[1], d[2], d[3]};
        pp[1] = u32x4{d[4], d[5], d[6], d[7]};
      }
    }
  }
  __syncthreads();
  if (wv < 8) {
    const int cb = 128 * (q4 & 1) + 64 * (q4 >> 1);
    const char* tp = smem + O_TILE + c16 * TROW + cb * 2;
    char* op = out + ((size_t)n0 * 4 + c16) * 512 + (wv * 32 + 8 * q4) * 2;
    const u32 nrows = cntN * 4;
#pragma unroll 2
    for (int rb = 0; rb < MAXN * 4 / 16; ++rb) {
      f32x4 a0 = {0.f, 0.f, 0.f, 0.f}, a1 = {0.f, 0.f, 0.f, 0.f};
#pragma unroll
      for (int ks = 0; ks < 8; ++ks) {
        const u32x4 bf = *(const u32x4*)(tp + rb * 16 * TROW + ks * 16);
        a0 = MFMA16(__builtin_bit_cast(bf16x8, wf[ks][0]), __builtin_bit_cast(bf16x8, bf), a0);
        a1 = MFMA16(__builtin_bit_cast(bf16x8, wf[ks][1]), __builtin_bit_cast(bf16x8, bf), a1);
      }
      if ((u32)(rb * 16 + c16) < nrows) {
        const u32x4 o = {cvtpk(a0[0], a1[0]), cvtpk(a0[1], a1[1]), cvtpk(a0[2], a1[2]), cvtpk(a0[3], a1[3])};
        *(u32x4*)(op + (size_t)rb * 16 * 512) = o;
      }
    }
  }
  return;
    }
    int ln = tid0 & 63;
    asm volatile("" : "+v"(ln));
    f32x4* wsp = (f32x4*)ws + ((size_t)blockIdx.x * NW + wv) * 1024 + ln;
#pragma unroll
    for (int r = 0; r < 4; ++r)
#pragma unroll
      for (int i = 0; i < 4; ++i)
        wsp[(r * 4 + i) * 64] = f32x4{acc[r][4 * i], acc[r][4 * i + 1], acc[r][4 * i + 2], acc[r][4 * i + 3]};
    pos = end;
    first = false;
  }
}

}

namespace fast {
#define PF 0
constexpr int ANT = 640;
constexpr int BNT = 512;
constexpr int SEGW = 128;
constexpr int LCAP = 8 * SEGW + 48;
constexpr int TROW = 528;
constexpr int MAXE = 256 * ANT;
constexpr size_t W_SLOT = 0;
constexpr size_t W_OVN = W_SLOT + 256 * 256 * 64;
constexpr size_t W_WR = W_OVN + 1024;
constexpr size_t W_WO = W_WR + 8192;
constexpr size_t W_OVF = W_WO + 131072;
constexpr size_t W_END = W_OVF + (size_t)MAXE * 8;
constexpr u32 INV = 0xffffffffu;
constexpr int O_CNT = 0;
constexpr int O_BASE = O_CNT + 1024;
constexpr int O_DEG = O_BASE + 1024;
constexpr int O_RANK = O_DEG + 256;
constexpr int O_INV = O_RANK + 256;
constexpr int O_ZERO = O_INV + 256;
constexpr int O_MISC = O_ZERO + 64;
constexpr int O_WRF = O_MISC + 64;
constexpr int O_EXP = O_WRF + 8192;
constexpr int O_TMP = O_EXP + 8 * SEGW * 8;
constexpr int O_META = O_TMP + LCAP * 4;
constexpr int O_AT = O_META + LCAP * 4;
constexpr int PCAP = 8 * SEGW;
constexpr int O_EA = O_AT + PCAP * 32;
constexpr int O_TILE = O_AT;
constexpr int LDS_BYTES = O_TILE + 160 * TROW;
static_assert(O_EA + PCAP * 32 + 32 <= LDS_BYTES, "LDS");
static_assert(LDS_BYTES <= 163840, "LDS");
static_assert((O_AT % 16) == 0 && (O_EA % 16) == 0 && (O_TILE % 16) == 0 && (O_META % 16) == 0, "align");

#define DPPADD(t, ctrl, rmask, bc) t += (u32)__builtin_amdgcn_update_dpp(0, (int)(t), ctrl, rmask, 0xf, bc)
DI u32 scan16(u32 t) {
  DPPADD(t, 0x111, 0xf, true);
  DPPADD(t, 0x112, 0xf, true);
  DPPADD(t, 0x114, 0xf, true);
  DPPADD(t, 0x118, 0xf, true);
  return t;
}
DI u32 scan32(u32 t) { t = scan16(t); DPPADD(t, 0x142, 0xa, false); return t; }
DI u32 scan64(u32 t) { t = scan32(t); DPPADD(t, 0x143, 0xc, false); return t; }
DI void wavesync() { __builtin_amdgcn_fence(__ATOMIC_RELEASE, "wavefront"); __builtin_amdgcn_wave_barrier(); __builtin_amdgcn_fence(__ATOMIC_ACQUIRE, "wavefront"); }

__global__ __launch_bounds__(ANT) void bin_kernel(
    const int* __restrict__ src, const int* __restrict__ dst, const unsigned short* __restrict__ w_r,
    const unsigned short* __restrict__ w_out, char* __restrict__ ws, const char* __restrict__ x,
    const char* __restrict__ sh, const char* __restrict__ eattr, int N, int E, int S, float invN) {
  __shared__ __attribute__((aligned(16))) u32 s_cnt[260];
  __shared__ __attribute__((aligned(16))) u32x4 s_img[1024];
  const int tid0 = threadIdx.x, tid = tid0, lane = tid & 63;
  const int wv = __builtin_amdgcn_readfirstlane(tid >> 6);
  const int i = blockIdx.x;
  ST(0);
  const int e = i * S + tid;
  const bool act = tid < S && e < E;
  const int ec = e < E ? e : E - 1;
  u32 dn = (u32)dst[ec], sn = (u32)src[ec];
  if (tid < 260) s_cnt[tid] = 0u;
  s_img[tid] = u32x4{INV, INV, INV, INV};
  if (tid < 1024 - ANT) s_img[tid + ANT] = u32x4{INV, INV, INV, INV};
  if (wv == 9 && lane < 34) {
    const unsigned short* p;
    char* o;
    if (lane < 32) {
      const int id = i * 32 + lane;
      const int l2 = id & 63, h = (id >> 6) & 1, ks = (id >> 7) & 7, w8 = id >> 10;
      const int q4 = l2 >> 4, c16 = l2 & 15;
      p = w_out + (128 * (q4 & 1) + 64 * (q4 >> 1) + 8 * ks) * 256 + w8 * 32 + 2 * c16 + h;
      o = ws + W_WO + (size_t)id * 16;
    } else {
      const int id = i * 2 + (lane - 32);
      const int l2 = id & 63, r = id >> 6;
      const int kq = l2 >> 4, j = l2 & 15;
      p = w_r + (8 * (kq & 1)) * 256 + 8 * (16 * (kq >> 1) + j) + r;
      o = ws + W_WR + (size_t)id * 16;
    }
    u32 v[8];
#pragma unroll
    for (int k = 0; k < 8; ++k) v[k] = p[k * 256];
    *(u32x4*)o = u32x4{v[0] | (v[1] << 16), v[2] | (v[3] << 16), v[4] | (v[5] << 16), v[6] | (v[7] << 16)};
  }
  u32 pf0 = 0, pf1 = 0, pf2 = 0;
  if (PF & 2) {
    pf1 = *(const u32*)(eattr + (size_t)ec * 32);
    pf2 = *(const u32*)(sh + (size_t)ec * 8);
  }
  if (PF & 1) {
    const u32 g = umin((u32)(i * ANT + tid), (u32)N * ((PF & 16) ? 8u : 16u) - 1u);
    pf0 = *(const u32*)(x + (size_t)g * 128);
  }
  __syncthreads();
  asm volatile("" : "+v"(dn), "+v"(sn));
  u32 b = (u32)((float)(dn * 256u + 255u) * invN);
  b = umin(b, 255u);
  if ((((b + 1u) * (u32)N) >> 8) <= dn) b += 1u;
  if (((b * (u32)N) >> 8) > dn) b -= 1u;
  b = umin(b, 255u);
  const u32 loc = dn - ((b * (u32)N) >> 8);
  if (act) {
    const u32 rec = (sn << 16) | ((u32)tid << 6) | loc;
    const u32 rk = atomicAdd(&s_cnt[b], 1u);
    if (rk < 16u) {
      ((u32*)s_img)[b * 16u + rk] = rec;
    } else {
      const u32 o = atomicAdd(&s_cnt[256], 1u);
      ((u32x2*)(ws + W_OVF))[i * ANT + o] = u32x2{rec, b};
    }
  }
  __syncthreads();
  u32x4* po = (u32x4*)(ws + W_SLOT) + i * 4;
  po[(tid >> 2) * 1024 + (tid & 3)] = s_img[tid];
  if (tid < 1024 - ANT) po[((tid + ANT) >> 2) * 1024 + (tid & 3)] = s_img[tid + ANT];
  if (tid == ANT - 1) ((u32*)(ws + W_OVN))[i] = s_cnt[256];
  if (PF & 3) asm volatile("" ::"v"(pf0), "v"(pf1), "v"(pf2));
  ST(1);
}

struct Ctx {
  const char* xb;
  u32 xoff;
  u32 js;
  u32 atl;
  u32 mad;
  u32 ead, east;
  u32 eao;
  u32 wra;
};

DI void load_x(const Ctx& c, u32x2 m, u32x4 (&X)[8]) {
#pragma unroll
  for (int i = 0; i < 2; ++i) {
    const char* p = c.xb + (size_t)(((m[i] >> 18) << 11) | c.xoff);
#pragma unroll
    for (int u = 0; u < 4; ++u) X[4 * i + u] = *(const u32x4*)(p + u * 512);
  }
}

DI f32x4 rwm(char* smem, const Ctx& c, const u32x4& eaf, int r) {
  const u32x4 w = *(const u32x4*)(smem + c.wra + r * 1024);
  const f32x4 z4 = {0.f, 0.f, 0.f, 0.f};
  return MFMA16(__builtin_bit_cast(bf16x8, eaf), __builtin_bit_cast(bf16x8, w), z4);
}

template <int PH>
DI void step(char* smem, const Ctx& c, int g, u32x2& m0, u32x2& m1, u32x4& eaf, f32x4 (&rw)[8],
             const u32x4 (&X)[8], u32x4 (&Y)[8], f32x16 (&acc)[8]) {
  u32x4 bt;
  u32 me = 0;
#pragma unroll
  for (int i = 0; i < 2; ++i) {
    const u32 a = ((m0[i] & 15u) == c.js) ? c.atl + (m0[i] & 0x7ff0u) * 2u : (u32)O_ZERO;
    const u32x2 t = *(const u32x2*)(smem + a);
    bt[2 * i] = t.x;
    bt[2 * i + 1] = t.y;
  }
  if (PH != 2) {
    asm volatile("" ::"v"(X[0]), "v"(X[1]), "v"(X[2]), "v"(X[3]), "v"(X[4]), "v"(X[5]), "v"(X[6]), "v"(X[7]));
    load_x(c, m1, Y);
    m0 = m1;
    m1 = *(const u32x2*)(smem + c.mad + (g + 2) * 16);
    __builtin_amdgcn_sched_barrier(0);
  }
#pragma unroll
  for (int r = 0; r < 8; ++r) {
    if (PH == 0 && r < 6) rw[r + 2] = rwm(smem, c, eaf, r + 2);
    if (PH == 1 && r == 2) me = *(const u32*)(smem + c.ead + ((g + 1) >> 1) * c.east);
    if (PH == 1 && r == 4) eaf = *(const u32x4*)(smem + c.eao + (me & 0x7ff0u) * 2u);
    u32x4 z;
#pragma unroll
    for (int i = 0; i < 2; ++i) {
      const float s = rw[r][(PH == 1 ? 2 : 0) + i];
#pragma unroll
      for (int up = 0; up < 2; ++up) {
        const u32 a = X[4 * i + 2 * up][r >> 1], b = X[4 * i + 2 * up + 1][r >> 1];
        z[2 * i + up] = (r & 1) ? cvtpk(bfhi(a) * s, bfhi(b) * s) : cvtpk(bflo(a) * s, bflo(b) * s);
      }
    }
    acc[r] = MFMA32(__builtin_bit_cast(bf16x8, z), __builtin_bit_cast(bf16x8, bt), acc[r]);
    if (PH == 1 && r >= 6) rw[r - 6] = rwm(smem, c, eaf, r - 6);
  }
}

#define STAMP_LDS 0
#define STA(k) do { } while (0)
#define STDUMP() do { } while (0)
__global__ __launch_bounds__(BNT, 2) void agg_kernel(const char* __restrict__ x, const char* __restrict__ sh,
                                                     const char* __restrict__ eattr, const u32* __restrict__ cg2,
                                                     char* __restrict__ out, char* __restrict__ ws, int N, int S,
                                                     float bscale) {
  __shared__ __attribute__((aligned(16))) char smem[LDS_BYTES + STAMP_LDS];
  const int tid0 = threadIdx.x;
  const int wv = __builtin_amdgcn_readfirstlane(tid0 >> 6);
  const int b = blockIdx.x;
  const u32 n0 = (u32)(b * N) >> 8;
  const u32 cntN = ((u32)((b + 1) * N) >> 8) - n0;
  u32* s_cnt = (u32*)(smem + O_CNT);
  u32* s_base = (u32*)(smem + O_BASE);
  u32* s_deg = (u32*)(smem + O_DEG);
  u32* s_rank = (u32*)(smem + O_RANK);
  u32* s_inv = (u32*)(smem + O_INV);
  u32* s_misc = (u32*)(smem + O_MISC);
  u32* s_meta = (u32*)(smem + O_META);
  u32x2* s_exp = (u32x2*)(smem + O_EXP) + wv * SEGW;
  u32* s_tmp = (u32*)(smem + O_TMP);
  STA(2);

  {
    if (tid0 >= 256 && tid0 < 256 + 64) s_deg[tid0 - 256] = 0u;
    if (tid0 >= 384 && tid0 < 384 + 64) s_inv[tid0 - 384] = 0xffffffffu;
    if (tid0 >= 448 && tid0 < 448 + 32) ((u32*)(smem + O_ZERO))[tid0 - 448] = (tid0 == 448 + 18) ? (u32)PCAP << 4 : 0u;
  }

  f32x16 acc[8];
#pragma unroll
  for (int r = 0; r < 8; ++r) {
#pragma unroll
    for (int k = 0; k < 16; ++k) {
      float z = 0.f;
      asm volatile("" : "+v"(z));
      acc[r][k] = z;
    }
  }

  int nseg = 1, nseg0 = 1, cps = 1;
  for (int seg = 0;; ++seg) {
    int tid = tid0;
    asm volatile("" : "+v"(tid));
    const int lane = tid & 63;
    const int q4 = lane >> 4, c16 = lane & 15;
    u32x4 cd[2];
    u32 eb;
    int sb = seg * SEGW;
    u32 ovn = 0;
    if (seg < nseg0) {
      const u32x4* sp = (const u32x4*)(ws + W_SLOT) + (b * 512 + tid) * 2;
      cd[0] = sp[0];
      cd[1] = sp[1];
      ovn = ((const u32*)(ws + W_OVN))[tid & 255];
      eb = (u32)((tid >> 1) * S);
    } else {
      const int idx = (seg - nseg0) * 8 + wv;
      const int sl = idx / cps, k0 = (idx - sl * cps) * SEGW + lane;
      const int n = (int)((const u32*)(ws + W_OVN))[sl];
      cd[0] = u32x4{INV, INV, INV, INV};
      cd[1] = cd[0];
#pragma unroll
      for (int j = 0; j < 2; ++j)
        if (k0 + j * 64 < n) {
          const u32x2 o = ((const u32x2*)(ws + W_OVF))[sl * ANT + k0 + j * 64];
          if (o.y == (u32)b) cd[0][j] = o.x;
        }
      eb = (u32)(sl * S);
      sb = 0;
    }
    const u32x4 wr0 = *(const u32x4*)(ws + W_WR + tid * 16);
    u32 pf0 = 0, pf1 = 0;
    if ((PF & 4) && seg == 0) {
      const u32 g = (u32)(b * BNT + tid), lim = (u32)N * 16u, g0 = (PF & 16) ? (u32)N * 8u : 0u;
      if (g0 + g < lim) pf0 = *(const u32*)(x + (size_t)(g0 + g) * 128);
      if (!(PF & 16) && g + 256u * BNT < lim) pf1 = *(const u32*)(x + (size_t)(g + 256u * BNT) * 128);
    }
    if ((PF & 8) && seg == 0) {
      if (lane == 0) pf1 = *(const u32*)(out + (size_t)n0 * 2048 + wv * 8192);
    }
    if (tid < 256) s_cnt[tid] = 0u;
    __syncthreads();
    u32 tc = 0;
#pragma unroll
    for (int k = 0; k < 8; ++k) tc += (cd[k >> 2][k & 3] != INV) ? 1u : 0u;
    const u32 tinc = scan64(tc);
    const int T = __builtin_amdgcn_readlane((int)tinc, 63);
    STA(3);
    {
      u32 p = tinc - tc - (u32)sb;
#pragma unroll
      for (int k = 0; k < 8; ++k) {
        const u32 r = cd[k >> 2][k & 3];
        const bool v = r != INV;
        if (k >= 4 && __builtin_amdgcn_ballot_w64(v) == 0ull) break;
        if (v && p < (u32)SEGW) s_exp[p] = u32x2{((r >> 16) << 18) | (eb + ((r >> 6) & 1023u)), r & 63u};
        p += v ? 1u : 0u;
      }
    }
    int Tn = T - sb;
    Tn = Tn < 0 ? 0 : (Tn > SEGW ? SEGW : Tn);
    wavesync();
    u32x2 mt[2], shv[2];
    u32x4 e0[2], e1[2];
    bool act[2];
    u32 eid[2];
#pragma unroll
    for (int j = 0; j < 2; ++j) {
      act[j] = j * 64 + lane < Tn;
      mt[j] = s_exp[j * 64 + lane];
      eid[j] = act[j] ? (mt[j].x & 0x3ffffu) : 0u;
    }
    STA(4);
    if (seg == 0) {
#pragma unroll
      for (int j = 0; j < 2; ++j)
        if (act[j]) atomicAdd(&s_deg[mt[j].y], 1u);
      if (lane == 0) atomicMax(&s_misc[0], (u32)T);
      if (ovn) atomicMax(&s_misc[1], ovn);
    }
    *(u32x4*)(smem + O_WRF + tid * 16) = wr0;
    if (tid < 2) *(u32x4*)(smem + O_EA + PCAP * 32 + tid * 16) = u32x4{0u, 0u, 0u, 0u};
#pragma unroll
    for (int j = 0; j < 2; ++j) shv[j] = *(const u32x2*)(sh + (size_t)eid[j] * 8);
    if (seg == 0) {
      STA(5);
      __syncthreads();
      const u32 d = umin(s_deg[lane], 127u);
      u32 gl = 0u, gh = 0u, el = 0xffffffffu, eh = 0xffu;
#pragma unroll
      for (int bit = 6; bit >= 0; --bit) {
        const bool on = (d >> bit) & 1u;
        const u64 B = __builtin_amdgcn_ballot_w64(on);
        const u32 bl = (u32)B, bh = (u32)(B >> 32);
        gl |= on ? 0u : (el & bl);
        gh |= on ? 0u : (eh & bh);
        el &= on ? bl : ~bl;
        eh &= on ? bh : ~bh;
      }
      const u32 rk = __builtin_popcount(gl) + __builtin_popcount(gh) +
                     __builtin_amdgcn_mbcnt_hi(eh, __builtin_amdgcn_mbcnt_lo(el, 0u));
      s_rank[lane] = rk;
      nseg0 = (int)((__builtin_amdgcn_readfirstlane(s_misc[0]) + SEGW - 1) / SEGW);
      nseg0 = nseg0 < 1 ? 1 : nseg0;
      cps = (int)((__builtin_amdgcn_readfirstlane(s_misc[1]) + SEGW - 1) / SEGW);
      nseg = nseg0 + 32 * cps;
      if (wv == 0 && (u32)lane < cntN) {
        const u32 slot = rk >> 3, r8 = rk & 7u;
        s_inv[((slot & 1u) ? 7u - r8 : r8) * 8u + slot] = (u32)lane;
      }
      wavesync();
    }
    if (PF & 12) asm volatile("" ::"v"(pf0), "v"(pf1));
    STA(6);
    u32 key[2], rk[2], slot[2], comp[2];
#pragma unroll
    for (int j = 0; j < 2; ++j) {
      key[j] = 0u; rk[j] = 0u; slot[j] = 0u; comp[j] = 0u;
      if (j * 64 < Tn) {
        const u32 nr = s_rank[act[j] ? mt[j].y : 0u];
        slot[j] = nr >> 3;
        const u32 r8 = nr & 7u;
        const u32 bk = umin((u32)((float)(mt[j].x >> 18) * bscale), 31u);
        key[j] = ((slot[j] & 1u) ? 7u - r8 : r8) * 32u + bk;
        comp[j] = mt[j].x;
        if (act[j]) rk[j] = atomicAdd(&s_cnt[key[j]], 1u);
      }
    }
    e0[0] = *(const u32x4*)(eattr + (size_t)eid[0] * 32);
    e1[0] = *(const u32x4*)(eattr + (size_t)eid[0] * 32 + 16);
    __syncthreads();
    STA(7);
    u32 ls = 0, lc = 0;
    {
      const u32x4 c = *(const u32x4*)(s_cnt + 4 * lane);
      const u32 p1 = c.x, p2 = p1 + c.y, p3 = p2 + c.z, tot = p3 + c.w;
      const u32 inc = scan64(tot);
      u32 run = 0, prev = 0, adj = 0;
#pragma unroll
      for (int k = 0; k < 8; ++k) {
        const u32 ek = __builtin_amdgcn_readlane(inc, 8 * k + 7);
        const u32 ck = ek - prev;
        ls = (wv == k) ? run : ls;
        lc = (wv == k) ? ck : lc;
        adj = ((lane >> 3) == k) ? run - prev : adj;
        run += ck ? ((ck + 3u) & ~3u) : 4u;
        prev = ek;
      }
      const u32 ex = inc - tot + adj;
      *(u32x4*)(s_base + 4 * lane) = u32x4{ex, ex + p1, ex + p2, ex + p3};
      wavesync();
    }
    u32 kb[2], kn[2];
#pragma unroll
    for (int j = 0; j < 2; ++j) {
      kb[j] = 0u;
      kn[j] = 0u;
      if (j * 64 < Tn) {
        kb[j] = s_base[key[j]];
        kn[j] = act[j] ? s_cnt[key[j]] : 0u;
        if (act[j]) s_tmp[kb[j] + rk[j]] = comp[j];
      }
    }
    e0[1] = *(const u32x4*)(eattr + (size_t)eid[1] * 32);
    e1[1] = *(const u32x4*)(eattr + (size_t)eid[1] * 32 + 16);
    __syncthreads();
    STA(8);
    {
      u32 p[2] = {kb[0], kb[1]};
      const u32 nm = kn[0] > kn[1] ? kn[0] : kn[1];
#pragma clang loop unroll(disable) vectorize(disable) interleave(disable)
      for (u32 k = 0; k < nm; k += 4) {
        u32 t[2][4];
#pragma unroll
        for (int j = 0; j < 2; ++j)
#pragma unroll
          for (int q = 0; q < 4; ++q) t[j][q] = s_tmp[kb[j] + k + q];
#pragma unroll
        for (int j = 0; j < 2; ++j)
#pragma unroll
          for (int q = 0; q < 4; ++q) p[j] += (k + q < kn[j] && t[j][q] < comp[j]) ? 1u : 0u;
      }
#pragma unroll
      for (int j = 0; j < 2; ++j)
        if (act[j]) s_meta[p[j]] = (comp[j] & 0xfffc0000u) | ((u32)(wv * SEGW + j * 64 + lane) << 4) | slot[j];
    }
    const u32 lp = lc ? ((lc + 3u) & ~3u) : 4u;
    if (lane < 4 && lc + lane < lp) {
      const u32 p = ls + lc + lane;
      const u32 sl = lc ? (s_tmp[ls + lc - 1] & 0xfffc0000u) : 0u;
      s_meta[p] = sl | ((u32)PCAP << 4) | 15u;
    }
    __syncthreads();
    STA(9);

    u32x4 wf[8][2];
    {
      const int ng = (int)(lp >> 2);
      const int h = lane >> 5, j = lane & 31;
      Ctx c;
      c.xb = x;
      c.xoff = j * 16;
      c.js = j >> 2;
      c.atl = O_AT + (j & 3) * 8;
      c.mad = O_META + (ls + 2 * h) * 4;
      const bool match = (q4 >> 1) == ((c16 >> 2) & 1);
      c.ead = match ? O_META + (ls + 4 * ((c16 >> 1) & 1) + 2 * (c16 >> 3) + (c16 & 1)) * 4 : O_MISC + 8;
      c.east = match ? 32 : 0;
      c.eao = O_EA + 16 * (q4 & 1);
      c.wra = O_WRF + lane * 16;
      u32x2 m0 = *(const u32x2*)(smem + c.mad);
      u32x2 m1 = *(const u32x2*)(smem + c.mad + 16);
      u32x4 XA[8], XB[8];
      load_x(c, m0, XA);
      const u32 me0 = *(const u32*)(smem + c.ead);
      __builtin_amdgcn_sched_barrier(0);
      u32x4 at[2][2];
      u64 cga = (u64)cg2;
      asm volatile("" : "+s"(cga));
      const __attribute__((address_space(4))) u32* cgp = (const __attribute__((address_space(4))) u32*)cga;
#pragma unroll
      for (int j = 0; j < 2; ++j) {
        at[j][0] = u32x4{0u, 0u, 0u, 0u};
        at[j][1] = at[j][0];
        if (j * 64 < Tn) {
          const float s0 = bflo(shv[j].x), s1 = bfhi(shv[j].x), s2 = bflo(shv[j].y), s3 = bfhi(shv[j].y);
#pragma unroll
          for (int w = 0; w < 4; ++w) {
            float a[4];
#pragma unroll
            for (int u = 0; u < 4; ++u) {
              const u32 c0 = cgp[u * 8 + 0 + (w >> 1)], c1 = cgp[u * 8 + 2 + (w >> 1)];
              const u32 c2 = cgp[u * 8 + 4 + (w >> 1)], c3 = cgp[u * 8 + 6 + (w >> 1)];
              a[u] = ((w & 1) ? bfhi(c0) : bflo(c0)) * s0 + ((w & 1) ? bfhi(c1) : bflo(c1)) * s1 +
                     ((w & 1) ? bfhi(c2) : bflo(c2)) * s2 + ((w & 1) ? bfhi(c3) : bflo(c3)) * s3;
            }
            at[j][w >> 1][2 * (w & 1)] = cvtpk(a[0], a[1]);
            at[j][w >> 1][2 * (w & 1) + 1] = cvtpk(a[2], a[3]);
          }
        }
      }
#pragma unroll
      for (int j = 0; j < 2; ++j) {
        if (j * 64 < Tn) {
          const int p = wv * SEGW + j * 64 + lane;
          u32x4* pa = (u32x4*)(smem + O_AT + p * 32);
          u32x4* pe = (u32x4*)(smem + O_EA + p * 32);
          pa[0] = at[j][0];
          pa[1] = at[j][1];
          pe[0] = e0[j];
          pe[1] = e1[j];
        }
      }
      __syncthreads();
      f32x4 rw[8];
      u32x4 eaf = *(const u32x4*)(smem + c.eao + (me0 & 0x7ff0u) * 2u);
#pragma unroll
      for (int r = 2; r < 8; ++r) rw[r] = f32x4{0.f, 0.f, 0.f, 0.f};
      rw[0] = rwm(smem, c, eaf, 0);
      rw[1] = rwm(smem, c, eaf, 1);
      int g = 0;
      for (; g + 2 < ng; g += 2) {
        step<0>(smem, c, g, m0, m1, eaf, rw, XA, XB, acc);
        step<1>(smem, c, g + 1, m0, m1, eaf, rw, XB, XA, acc);
      }
      if (g + 1 < ng) {
        step<0>(smem, c, g, m0, m1, eaf, rw, XA, XB, acc);
        ++g;
#pragma unroll
        for (int k = 0; k < 8; ++k) XA[k] = XB[k];
#pragma unroll
        for (int r = 0; r < 8; ++r) { rw[r][0] = rw[r][2]; rw[r][1] = rw[r][3]; }
      } else {
#pragma unroll
        for (int r = 2; r < 8; ++r) rw[r] = rwm(smem, c, eaf, r);
      }
      step<2>(smem, c, g, m0, m1, eaf, rw, XA, XB, acc);
      __builtin_amdgcn_sched_barrier(0);
      {
        const char* wp = ws + W_WO + (size_t)(wv * 16 * 64 + lane) * 16;
#pragma unroll
        for (int ks = 0; ks < 8; ++ks) {
          wf[ks][0] = *(const u32x4*)(wp + (ks * 2 + 0) * 1024);
          wf[ks][1] = *(const u32x4*)(wp + (ks * 2 + 1) * 1024);
        }
        __builtin_amdgcn_sched_barrier(0);
      }
    }
    STA(10);
    __syncthreads();
    if (seg + 1 < nseg) continue;

    {
      const u32 loc = s_inv[wv * 8 + ((lane & 31) >> 2)];
      if (loc != 0xffffffffu) {
        char* rowp = smem + O_TILE + (loc * 4 + (lane & 3)) * TROW + (lane >> 5) * 64;
#pragma unroll
        for (int a = 0; a < 16; ++a)
          *(u32x4*)(rowp + (a >> 2) * 128 + (a & 3) * 16) =
              u32x4{cvtpk(acc[0][a], acc[1][a]), cvtpk(acc[2][a], acc[3][a]), cvtpk(acc[4][a], acc[5][a]),
                    cvtpk(acc[6][a], acc[7][a])};
      }
    }
    STA(11);
    __syncthreads();
    STA(12);
    {
      const int cb = 128 * (q4 & 1) + 64 * (q4 >> 1);
      const char* tp = smem + O_TILE + c16 * TROW + cb * 2;
      char* op = out + ((size_t)n0 * 4 + c16) * 512 + (wv * 32 + 8 * q4) * 2;
      const u32 nrows = cntN * 4;
#pragma unroll 2
      for (int rb = 0; rb < 10; ++rb) {
        f32x4 a0 = {0.f, 0.f, 0.f, 0.f}, a1 = {0.f, 0.f, 0.f, 0.f};
#pragma unroll
        for (int ks = 0; ks < 8; ++ks) {
          const u32x4 bf = *(const u32x4*)(tp + rb * 16 * TROW + ks * 16);
          a0 = MFMA16(__builtin_bit_cast(bf16x8, wf[ks][0]), __builtin_bit_cast(bf16x8, bf), a0);
          a1 = MFMA16(__builtin_bit_cast(bf16x8, wf[ks][1]), __builtin_bit_cast(bf16x8, bf), a1);
        }
        if ((u32)(rb * 16 + c16) < nrows) {
          const u32x4 o = {cvtpk(a0[0], a1[0]), cvtpk(a0[1], a1[1]), cvtpk(a0[2], a1[2]), cvtpk(a0[3], a1[3])};
          *(u32x4*)(op + (size_t)rb * 16 * 512) = o;
        }
      }
    }
    STA(13);
    STDUMP();
    return;
  }
}
}

extern "C" void kernel_launch(void* const* d_in, const int* in_sizes, int n_in, void* d_out, int out_size,
                              void* d_ws, size_t ws_size, hipStream_t stream) {
  const int N = in_sizes[0] / 1024;
  const int E = in_sizes[1];
  if (N <= 0) return;
  if (N <= 10240 && E >= 1 && E <= fast::MAXE && ws_size >= fast::W_END) {
    int S = (E + 255) / 256;
    if (S < 1) S = 1;
    fast::bin_kernel<<<256, fast::ANT, 0, stream>>>((const int*)d_in[1], (const int*)d_in[2],
                                                    (const unsigned short*)d_in[6], (const unsigned short*)d_in[7],
                                                    (char*)d_ws, (const char*)d_in[0], (const char*)d_in[3],
                                                    (const char*)d_in[4], N, E, S, 1.0f / (float)N);
    fast::agg_kernel<<<256, fast::BNT, 0, stream>>>((const char*)d_in[0], (const char*)d_in[3], (const char*)d_in[4],
                                                    (const u32*)d_in[5], (char*)d_out, (char*)d_ws, N, S,
                                                    32.0f / (float)N);
    return;
  }
  int G = (N + gen::MAXN - 1) / gen::MAXN;
  if (G < 256) G = 256;
  if (G > N) G = N;
  size_t gmax = ws_size / ((size_t)gen::NW * 16384);
  if (gmax > 65536) gmax = 65536;
  if (gmax < 1) return;
  for (int b0 = 0; b0 < G; b0 += (int)gmax) {
    const int g = (G - b0 < (int)gmax) ? G - b0 : (int)gmax;
    gen::mace_kernel<<<g, gen::NT, 0, stream>>>(
        (const char*)d_in[0], (const int*)d_in[1], (const int*)d_in[2], (const char*)d_in[3], (const char*)d_in[4],
        (const unsigned short*)d_in[5], (const char*)d_in[6], (const char*)d_in[7], (char*)d_out, (float*)d_ws, N, E,
        b0, G, (float)gen::NB / (float)N);
  }
}
```

```cpp
#include <hip/hip_runtime.h>
#include <hip/hip_bf16.h>
#include <stdint.h>

typedef unsigned int u32;
typedef unsigned long long u64;
typedef __attribute__((ext_vector_type(8))) short bf16x8;
typedef __attribute__((ext_vector_type(2))) __bf16 bf16x2_t;
typedef __attribute__((ext_vector_type(2))) float f32x2;
typedef __attribute__((ext_vector_type(4))) float f32x4;
typedef __attribute__((ext_vector_type(16))) float f32x16;
typedef __attribute__((ext_vector_type(2))) unsigned int u32x2;
typedef __attribute__((ext_vector_type(4))) unsigned int u32x4;

#define DI __device__ __forceinline__
#define ST(k) do { } while (0)
#define MFMA32(a, b, c) __builtin_amdgcn_mfma_f32_32x32x16_bf16((a), (b), (c), 0, 0, 0)
#define MFMA16(a, b, c) __builtin_amdgcn_mfma_f32_16x16x32_bf16((a), (b), (c), 0, 0, 0)

DI u32 cvtpk(float a, float b) {
  f32x2 v = {a, b};
  bf16x2_t r = __builtin_convertvector(v, bf16x2_t);
  return __builtin_bit_cast(u32, r);
}
DI float bflo(u32 v) { return __builtin_bit_cast(float, v << 16); }
DI float bfhi(u32 v) { return __builtin_bit_cast(float, v & 0xffff0000u); }
DI u32 mbcnt(u64 m) { return __builtin_amdgcn_mbcnt_hi((u32)(m >> 32), __builtin_amdgcn_mbcnt_lo((u32)m, 0u)); }
DI u32 umin(u32 a, u32 b) { return a < b ? a : b; }

namespace gen {
constexpr int NT = 768, NW = 12, NRG = 6, NB = 32;
constexpr int MAXN = 40;
constexpr int CAP = 944;
constexpr int LCAP = CAP + 48;
constexpr int CAPW = 128;
constexpr int WMIN = 512;
constexpr int SU = 9;
constexpr int TROW = 528;
constexpr int PFROWS = 1536;

constexpr int O_CNT = 0;
constexpr int O_BASE = O_CNT + 192 * 4;
constexpr int O_DEG = O_BASE + 192 * 4;
constexpr int O_MAP = O_DEG + 64 * 4;
constexpr int O_INV = O_MAP + 64 * 4;
constexpr int O_CGT = O_INV + 64 * 4;
constexpr int O_ZERO = O_CGT + 64 * 4;
constexpr int O_MISC = O_ZERO + 64;
constexpr int O_WR = O_MISC + 64;
constexpr int O_META = O_WR + 8192;
constexpr int O_AT = O_META + LCAP * 4;
constexpr int O_EA = O_AT + LCAP * 32;
constexpr int O_TILE = O_EA + LCAP * 32;
constexpr int O_CANDQ = O_TILE;
constexpr int O_CANDI = O_CANDQ + NW * CAPW * 16;
constexpr int O_HITE = O_CANDI + NW * CAPW * 4;
constexpr int O_HITL = O_HITE + 1024 * 4;
constexpr int O_TMP = O_HITL + 1024 * 4;
constexpr int LDS_BYTES = O_TILE + MAXN * 4 * TROW;
static_assert(O_TMP + LCAP * 8 <= LDS_BYTES, "alias region too small");
static_assert(LDS_BYTES <= 163840, "LDS");
static_assert((O_AT % 16) == 0 && (O_EA % 16) == 0 && (O_TILE % 16) == 0 && (O_WR % 16) == 0, "align");

struct MainCtx {
  const char* xb;
  u32 xoff;
  u32 myslot;
  u32 atlane;
  u32 metaaddr;
  u32 eaaddr, eastride;
  u32 wraddr;
};

DI void load_block(const char* xb, u32 xoff, u32 m0, u32 m1, u32x2 (&buf)[8]) {
  const u32 o0 = (m0 & 0xfffff800u) | xoff, o1 = (m1 & 0xfffff800u) | xoff;
  const char* p0 = xb + (size_t)o0;
  const char* p1 = xb + (size_t)o1;
#pragma unroll
  for (int u = 0; u < 4; ++u) buf[u] = *(const u32x2*)(p0 + u * 512);
#pragma unroll
  for (int u = 0; u < 4; ++u) buf[4 + u] = *(const u32x2*)(p1 + u * 512);
}

DI u32x4 make_z(const u32x2 (&buf)[8], int r, float s0, float s1) {
  u32x4 z;
#pragma unroll
  for (int i = 0; i < 2; ++i) {
    const float s = i ? s1 : s0;
#pragma unroll
    for (int up = 0; up < 2; ++up) {
      const u32 a = (r & 2) ? buf[4 * i + 2 * up].y : buf[4 * i + 2 * up].x;
      const u32 b = (r & 2) ? buf[4 * i + 2 * up + 1].y : buf[4 * i + 2 * up + 1].x;
      z[2 * i + up] = (r & 1) ? cvtpk(bfhi(a) * s, bfhi(b) * s) : cvtpk(bflo(a) * s, bflo(b) * s);
    }
  }
  return z;
}

template <bool PF>
DI void step(char* smem, const MainCtx& c, int g, u32x4& mc, u32x4& eaf, u32x2 (&xb0)[8], u32x2 (&xb1)[8],
             f32x16 (&acc)[4]) {
  const u32 ab = c.atlane + g * 256;
  u32x2 bt[4];
#pragma unroll
  for (int k = 0; k < 4; ++k) {
    const u32 a = ((mc[k] & 15u) == c.myslot) ? ab + k * 32 : (u32)O_ZERO;
    bt[k] = *(const u32x2*)(smem + a);
  }
  if (PF) mc = *(const u32x4*)(smem + c.metaaddr + (g + 1) * 32);
  f32x4 rw[4];
  const f32x4 z4 = {0.f, 0.f, 0.f, 0.f};
  u32 wra = c.wraddr;
  asm volatile("" : "+v"(wra));
#pragma unroll
  for (int r = 0; r < 4; ++r) {
    const u32x4 wr = *(const u32x4*)(smem + wra + r * 1024);
    rw[r] = MFMA16(__builtin_bit_cast(bf16x8, eaf), __builtin_bit_cast(bf16x8, wr), z4);
  }
  if (PF) eaf = *(const u32x4*)(smem + c.eaaddr + (g + 1) * c.eastride);
  {
    const u32x4 b0 = {bt[0].x, bt[0].y, bt[1].x, bt[1].y};
#pragma unroll
    for (int r = 0; r < 4; ++r) {
      const u32x4 z = make_z(xb0, r, rw[r][0], rw[r][1]);
      acc[r] = MFMA32(__builtin_bit_cast(bf16x8, z), __builtin_bit_cast(bf16x8, b0), acc[r]);
    }
    if (PF) load_block(c.xb, c.xoff, mc[0], mc[1], xb0);
  }
  {
    const u32x4 b1 = {bt[2].x, bt[2].y, bt[3].x, bt[3].y};
#pragma unroll
    for (int r = 0; r < 4; ++r) {
      const u32x4 z = make_z(xb1, r, rw[r][2], rw[r][3]);
      acc[r] = MFMA32(__builtin_bit_cast(bf16x8, z), __builtin_bit_cast(bf16x8, b1), acc[r]);
    }
    if (PF) load_block(c.xb, c.xoff, mc[2], mc[3], xb1);
  }
}

__global__ __launch_bounds__(NT, 3) void mace_kernel(
    const char* __restrict__ x, const int* __restrict__ src, const int* __restrict__ dst,
    const char* __restrict__ sh, const char* __restrict__ eattr, const unsigned short* __restrict__ cg,
    const char* __restrict__ w_r, const char* __restrict__ w_out, char* __restrict__ out,
    float* __restrict__ ws, int N, int E, int b0, int G, float bscale) {
  __shared__ __attribute__((aligned(16))) char smem[LDS_BYTES];
  const int tid0 = threadIdx.x;
  const int wv = __builtin_amdgcn_readfirstlane(tid0 >> 6);
  const int rg = wv >> 1, hf = wv & 1;
  const int b = b0 + blockIdx.x;
  const u32 n0 = (u32)(((u64)b * (u64)N) / (u64)G);
  const u32 cntN = (u32)((((u64)b + 1) * (u64)N) / (u64)G) - n0;

  u32* s_cnt = (u32*)(smem + O_CNT);
  u32* s_base = (u32*)(smem + O_BASE);
  u32* s_deg = (u32*)(smem + O_DEG);
  u32* s_map = (u32*)(smem + O_MAP);
  u32* s_inv = (u32*)(smem + O_INV);
  float* s_cgt = (float*)(smem + O_CGT);
  u32* s_misc = (u32*)(smem + O_MISC);
  u32* s_hitE = (u32*)(smem + O_HITE);
  u32* s_hitL = (u32*)(smem + O_HITL);
  u64* s_tmp = (u64*)(smem + O_TMP);
  u32* s_meta = (u32*)(smem + O_META);

  if (wv < 2) {
    const int lane = tid0 & 63;
    const int q4 = lane >> 4, c16 = lane & 15;
    u32x4 wr[4];
    u32x2 L[8];
    const char* p = w_r + (size_t)(8 * (q4 & 1)) * 512 + (hf * 128 + 64 * (q4 >> 1) + 4 * c16) * 2;
#pragma unroll
    for (int j = 0; j < 8; ++j) L[j] = *(const u32x2*)(p + j * 512);
#pragma unroll
    for (int jj = 0; jj < 4; ++jj) {
      wr[0][jj] = __builtin_amdgcn_perm(L[2 * jj + 1].x, L[2 * jj].x, 0x05040100u);
      wr[1][jj] = __builtin_amdgcn_perm(L[2 * jj + 1].x, L[2 * jj].x, 0x07060302u);
      wr[2][jj] = __builtin_amdgcn_perm(L[2 * jj + 1].y, L[2 * jj].y, 0x05040100u);
      wr[3][jj] = __builtin_amdgcn_perm(L[2 * jj + 1].y, L[2 * jj].y, 0x07060302u);
    }
#pragma unroll
    for (int r = 0; r < 4; ++r) *(u32x4*)(smem + O_WR + hf * 4096 + r * 1024 + lane * 16) = wr[r];
  }
  if (wv == 2) {
    const int tid = tid0 & 63;
    const int w = tid >> 4, u = (tid >> 2) & 3, v = tid & 3;
    s_cgt[tid] = bflo((u32)cg[u * 16 + v * 4 + w]);
    s_map[tid] = (u32)(tid % NRG) | ((u32)(tid / NRG) << 8);
    const u32 l = (u32)((tid & 7) * NRG + (tid >> 3));
    s_inv[tid] = (tid < NRG * 8 && l < cntN) ? l : 0xffffffffu;
    if (tid < 16) ((u32*)(smem + O_ZERO))[tid] = 0u;
  }

  bool first = true, again = false;
  int pos = 0;
  int W = (E + 3) & ~3;
  for (;;) {
    if (again) __syncthreads();
    again = true;
    int tid = tid0;
    asm volatile("" : "+v"(tid));
    const int lane = tid & 63;
    const int q4 = lane >> 4, c16 = lane & 15, h = lane >> 5;
    u32 pfv = 0;
    {
      const u32 line = (u32)(b >> 3) * NT + tid;
      if (first && line < (u32)PFROWS * 16u && line < (u32)N * 16u) pfv = *(const u32*)(x + (size_t)line * 128);
    }
    if (tid < 192) s_cnt[tid] = 0u;
    if (tid >= 192 && tid < 256) s_deg[tid - 192] = 0u;
    if (tid >= 256 && tid < 258) s_misc[tid - 256] = 0u;
    __syncthreads();
    const int end = (E - pos < W) ? E : pos + W;

    {
      const u32x4* dq = (const u32x4*)dst;
      u32x4* cq = (u32x4*)(smem + O_CANDQ) + wv * CAPW;
      u32* ci = (u32*)(smem + O_CANDI) + wv * CAPW;
      const int q0 = pos >> 2, qf = end >> 2;
      u32 nc = 0;
      for (int qb = q0 + wv * 64; qb < qf; qb += NT * SU) {
        u32x4 v[SU];
#pragma unroll
        for (int k = 0; k < SU; ++k) {
          const int q = qb + k * NT + lane;
          v[k] = dq[q < qf ? q : qf - 1];
        }
#pragma unroll
        for (int k = 0; k < SU; ++k) {
          const int q = qb + k * NT + lane;
          const u32 m = umin(umin(v[k].x - n0, v[k].y - n0), umin(v[k].z - n0, v[k].w - n0));
          const bool hit = (m < cntN) && (q < qf);
          const u64 mask = __ballot(hit);
          if (mask) {
            const u32 p = nc + mbcnt(mask);
            if (hit && p < (u32)CAPW) { cq[p] = v[k]; ci[p] = (u32)q; }
            nc += (u32)__popcll(mask);
          }
        }
      }
      if (wv == NW - 1 && (end & 3)) {
        u32x4 v;
        const int e4 = qf * 4;
        v.x = (u32)dst[e4];
        v.y = (e4 + 1 < end) ? (u32)dst[e4 + 1] : n0 - 1u;
        v.z = (e4 + 2 < end) ? (u32)dst[e4 + 2] : n0 - 1u;
        v.w = n0 - 1u;
        if (lane == 0 && nc < (u32)CAPW) { cq[nc] = v; ci[nc] = (u32)qf; }
        nc += 1;
      }
      if (nc > (u32)CAPW) { if (lane == 0) s_misc[1] = 1u; nc = CAPW; }
      __builtin_amdgcn_wave_barrier();
      for (u32 i0 = 0; i0 < nc; i0 += 64) {
        const u32 i = i0 + lane;
        const bool valid = i < nc;
        const u32x4 v = cq[valid ? i : 0];
        const u32 q = ci[valid ? i : 0];
        const u32 t[4] = {v.x - n0, v.y - n0, v.z - n0, v.w - n0};
        u64 mk[4];
        u32 tot = 0;
#pragma unroll
        for (int k = 0; k < 4; ++k) { mk[k] = __ballot(valid && t[k] < cntN); tot += (u32)__popcll(mk[k]); }
        u32 bs = 0;
        if (lane == 0) bs = atomicAdd(&s_misc[0], tot);
        bs = __builtin_amdgcn_readfirstlane(bs);
#pragma unroll
        for (int k = 0; k < 4; ++k) {
          const u32 p = bs + mbcnt(mk[k]);
          if (valid && t[k] < cntN && p < (u32)CAP) {
            s_hitE[p] = 4 * q + k;
            s_hitL[p] = t[k];
            atomicAdd(&s_deg[t[k]], 1u);
          }
          bs += (u32)__popcll(mk[k]);
        }
      }
    }
    asm volatile("" ::"v"(pfv));
    __syncthreads();
    const u32 nh = __builtin_amdgcn_readfirstlane(s_misc[0]);
    if (nh > (u32)CAP || __builtin_amdgcn_readfirstlane(s_misc[1]) != 0u) {
      W = ((W >> 1) + 3) & ~3;
      if (W < WMIN) W = WMIN;
      continue;
    }

    u32 he[2], hl[2], hs[2];
    u32x2 hsh[2];
    u32x4 hea[2][2];
    bool act[2];
#pragma unroll
    for (int j = 0; j < 2; ++j) {
      const u32 i = tid + j * NT;
      act[j] = i < nh;
      he[j] = 0u; hl[j] = 0u; hs[j] = 0u;
      hsh[j] = u32x2{0u, 0u};
      hea[j][0] = u32x4{0u, 0u, 0u, 0u};
      hea[j][1] = u32x4{0u, 0u, 0u, 0u};
      if (j == 0 ? nh > 0u : nh > (u32)NT) {
        const u32 ii = act[j] ? i : 0;
        he[j] = s_hitE[ii];
        hl[j] = s_hitL[ii];
        hs[j] = (u32)src[he[j]];
        hsh[j] = *(const u32x2*)(sh + (size_t)he[j] * 8);
        hea[j][0] = *(const u32x4*)(eattr + (size_t)he[j] * 32);
        hea[j][1] = *(const u32x4*)(eattr + (size_t)he[j] * 32 + 16);
      }
    }
    if (first && wv == NW - 1) {
      const u32 d = s_deg[lane];
      u32 rank = 0;
      for (u32 j = 0; j < cntN; ++j) {
        const u32 dj = __builtin_amdgcn_readlane(d, j);
        rank += (dj > d || (dj == d && j < (u32)lane)) ? 1u : 0u;
      }
      const u32 slot = rank / NRG, r = rank % NRG;
      const u32 g = (slot & 1) ? (NRG - 1 - r) : r;
      s_inv[lane] = 0xffffffffu;
      __builtin_amdgcn_wave_barrier();
      if ((u32)lane < cntN) { s_map[lane] = g | (slot << 8); s_inv[g * 8 + slot] = lane; }
    }
    __syncthreads();

    u32 key[2], rk[2], mp[2];
#pragma unroll
    for (int j = 0; j < 2; ++j) {
      key[j] = 0u; rk[j] = 0u; mp[j] = 0u;
      if (j == 0 ? nh > 0u : nh > (u32)NT) {
        mp[j] = s_map[hl[j]];
        u32 bk = (u32)((float)hs[j] * bscale);
        bk = umin(bk, NB - 1);
        key[j] = (mp[j] & 255u) * NB + bk;
        if (act[j]) rk[j] = atomicAdd(&s_cnt[key[j]], 1u);
      }
    }
    __syncthreads();
    u32 ls = 0, lc = 0;
    {
      u32 v[3], inc[3];
#pragma unroll
      for (int k = 0; k < 3; ++k) {
        v[k] = s_cnt[k * 64 + lane];
        u32 t = v[k];
        t += (u32)__builtin_amdgcn_update_dpp(0, (int)t, 0x111, 0xf, 0xf, true);
        t += (u32)__builtin_amdgcn_update_dpp(0, (int)t, 0x112, 0xf, 0xf, true);
        t += (u32)__builtin_amdgcn_update_dpp(0, (int)t, 0x114, 0xf, 0xf, true);
        t += (u32)__builtin_amdgcn_update_dpp(0, (int)t, 0x118, 0xf, 0xf, true);
        t += (u32)__builtin_amdgcn_update_dpp(0, (int)t, 0x142, 0xa, 0xf, false);
        inc[k] = t;
      }
      u32 run = 0;
#pragma unroll
      for (int k = 0; k < 3; ++k) {
        const u32 c0 = __builtin_amdgcn_readlane(inc[k], 31);
        const u32 c1 = __builtin_amdgcn_readlane(inc[k], 63);
        const u32 s0 = run, s1 = run + ((c0 + 7u) & ~7u);
        run = s1 + ((c1 + 7u) & ~7u);
        ls = (rg == 2 * k) ? s0 : ls;
        lc = (rg == 2 * k) ? c0 : lc;
        ls = (rg == 2 * k + 1) ? s1 : ls;
        lc = (rg == 2 * k + 1) ? c1 : lc;
        s_base[k * 64 + lane] = (h ? s1 : s0) + inc[k] - v[k];
      }
      __builtin_amdgcn_wave_barrier();
    }
    u32 kb[2];
    u64 comp[2];
#pragma unroll
    for (int j = 0; j < 2; ++j) {
      kb[j] = 0u; comp[j] = 0ull;
      if (j == 0 ? nh > 0u : nh > (u32)NT) {
        kb[j] = s_base[key[j]];
        comp[j] = ((u64)hs[j] << 32) | he[j];
        if (act[j]) s_tmp[kb[j] + rk[j]] = comp[j];
      }
    }
    __syncthreads();
#pragma unroll
    for (int j = 0; j < 2; ++j) {
      if ((j == 0 || nh > (u32)NT) && act[j]) {
        const u32 n = s_cnt[key[j]];
        u32 p = kb[j];
        for (u32 k = 0; k < n; ++k) p += (s_tmp[kb[j] + k] < comp[j]) ? 1u : 0u;
        s_meta[p] = (hs[j] << 11) | (mp[j] >> 8);
        const float s0 = bflo(hsh[j].x), s1 = bfhi(hsh[j].x), s2 = bflo(hsh[j].y), s3 = bfhi(hsh[j].y);
        u32 at[8];
#pragma unroll
        for (int w = 0; w < 4; ++w) {
          float a[4];
#pragma unroll
          for (int u = 0; u < 4; ++u) {
            const f32x4 cv = *(const f32x4*)(s_cgt + w * 16 + u * 4);
            a[u] = cv.x * s0 + cv.y * s1 + cv.z * s2 + cv.w * s3;
          }
          at[2 * w] = cvtpk(a[0], a[1]);
          at[2 * w + 1] = cvtpk(a[2], a[3]);
        }
        u32x4* pa = (u32x4*)(smem + O_AT + p * 32);
        pa[0] = u32x4{at[0], at[1], at[2], at[3]};
        pa[1] = u32x4{at[4], at[5], at[6], at[7]};
        u32x4* pe = (u32x4*)(smem + O_EA + p * 32);
        pe[0] = hea[j][0];
        pe[1] = hea[j][1];
      }
    }
    if (hf == 0 && lc > 0) {
      const u32 p = ls + lc + lane;
      if (p < ls + ((lc + 7u) & ~7u)) {
        const u32 sl = (u32)(s_tmp[ls + lc - 1] >> 32);
        s_meta[p] = (sl << 11) | 15u;
        const u32x4 zz = {0u, 0u, 0u, 0u};
        u32x4* pe = (u32x4*)(smem + O_EA + p * 32);
        pe[0] = zz;
        pe[1] = zz;
      }
    }
    __syncthreads();

    f32x16 acc[4];
    if (first) {
#pragma unroll
      for (int r = 0; r < 4; ++r)
#pragma unroll
        for (int i = 0; i < 16; ++i) acc[r][i] = 0.f;
    } else {
      const f32x4* wsp = (const f32x4*)ws + ((size_t)blockIdx.x * NW + wv) * 1024 + lane;
#pragma unroll
      for (int r = 0; r < 4; ++r)
#pragma unroll
        for (int i = 0; i < 4; ++i) {
          const f32x4 t = wsp[(r * 4 + i) * 64];
          acc[r][4 * i] = t.x; acc[r][4 * i + 1] = t.y; acc[r][4 * i + 2] = t.z; acc[r][4 * i + 3] = t.w;
        }
    }
    const int ng = (int)((lc + 7u) >> 3);
    if (ng > 0) {
      MainCtx c;
      c.xb = x;
      c.xoff = hf * 256 + (lane & 31) * 8;
      c.myslot = (lane & 31) >> 2;
      c.atlane = O_AT + (lane & 3) * 8 + (ls + 4 * h) * 32;
      c.metaaddr = O_META + (ls + 4 * h) * 4;
      const bool match = (q4 >> 1) == ((c16 >> 2) & 1);
      c.eaaddr = match ? O_EA + (ls + 4 * (c16 >> 3) + (c16 & 3)) * 32 + 16 * (q4 & 1) : O_ZERO;
      c.eastride = match ? 256 : 0;
      c.wraddr = O_WR + hf * 4096 + lane * 16;
      u32x4 mc = *(const u32x4*)(smem + c.metaaddr);
      u32x4 eaf = *(const u32x4*)(smem + c.eaaddr);
      u32x2 xb0[8], xb1[8];
      load_block(c.xb, c.xoff, mc[0], mc[1], xb0);
      load_block(c.xb, c.xoff, mc[2], mc[3], xb1);
      int g = 0;
      for (; g < ng - 1; ++g) step<true>(smem, c, g, mc, eaf, xb0, xb1, acc);
      step<false>(smem, c, g, mc, eaf, xb0, xb1, acc);
    }
    if (end >= E) {

  u32x4 wf[8][2];
#pragma unroll
  for (int ks = 0; ks < 8; ++ks) { wf[ks][0] = u32x4{0u, 0u, 0u, 0u}; wf[ks][1] = u32x4{0u, 0u, 0u, 0u}; }
  if (wv < 8) {
    const int cb = 128 * (q4 & 1) + 64 * (q4 >> 1);
    const char* p = w_out + (size_t)cb * 512 + (wv * 32 + 2 * c16) * 2;
#pragma unroll
    for (int ks = 0; ks < 8; ++ks) {
      u32 L[8];
#pragma unroll
      for (int j = 0; j < 8; ++j) L[j] = *(const u32*)(p + (ks * 8 + j) * 512);
#pragma unroll
      for (int jj = 0; jj < 4; ++jj) {
        wf[ks][0][jj] = __builtin_amdgcn_perm(L[2 * jj + 1], L[2 * jj], 0x05040100u);
        wf[ks][1][jj] = __builtin_amdgcn_perm(L[2 * jj + 1], L[2 * jj], 0x07060302u);
      }
    }
  }
  {
    const u32 loc = s_inv[rg * 8 + ((lane & 31) >> 2)];
    if (loc != 0xffffffffu) {
      char* rowp = smem + O_TILE + (loc * 4 + (lane & 3)) * TROW + (hf * 128 + 16 * h) * 2;
#pragma unroll
      for (int qq = 0; qq < 4; ++qq) {
        u32 d[8];
#pragma unroll
        for (int a = 0; a < 4; ++a) {
          d[2 * a] = cvtpk(acc[0][4 * qq + a], acc[1][4 * qq + a]);
          d[2 * a + 1] = cvtpk(acc[2][4 * qq + a], acc[3][4 * qq + a]);
        }
        u32x4* pp = (u32x4*)(rowp + qq * 64);
        pp[0] = u32x4{d[0], d[1], d[2], d[3]};
        pp[1] = u32x4{d[4], d[5], d[6], d[7]};
      }
    }
  }
  __syncthreads();
  if (wv < 8) {
    const int cb = 128 * (q4 & 1) + 64 * (q4 >> 1);
    const char* tp = smem + O_TILE + c16 * TROW + cb * 2;
    char* op = out + ((size_t)n0 * 4 + c16) * 512 + (wv * 32 + 8 * q4) * 2;
    const u32 nrows = cntN * 4;
#pragma unroll 2
    for (int rb = 0; rb < MAXN * 4 / 16; ++rb) {
      f32x4 a0 = {0.f, 0.f, 0.f, 0.f}, a1 = {0.f, 0.f, 0.f, 0.f};
#pragma unroll
      for (int ks = 0; ks < 8; ++ks) {
        const u32x4 bf = *(const u32x4*)(tp + rb * 16 * TROW + ks * 16);
        a0 = MFMA16(__builtin_bit_cast(bf16x8, wf[ks][0]), __builtin_bit_cast(bf16x8, bf), a0);
        a1 = MFMA16(__builtin_bit_cast(bf16x8, wf[ks][1]), __builtin_bit_cast(bf16x8, bf), a1);
      }
      if ((u32)(rb * 16 + c16) < nrows) {
        const u32x4 o = {cvtpk(a0[0], a1[0]), cvtpk(a0[1], a1[1]), cvtpk(a0[2], a1[2]), cvtpk(a0[3], a1[3])};
        *(u32x4*)(op + (size_t)rb * 16 * 512) = o;
      }
    }
  }
  return;
    }
    int ln = tid0 & 63;
    asm volatile("" : "+v"(ln));
    f32x4* wsp = (f32x4*)ws + ((size_t)blockIdx.x * NW + wv) * 1024 + ln;
#pragma unroll
    for (int r = 0; r < 4; ++r)
#pragma unroll
      for (int i = 0; i < 4; ++i)
        wsp[(r * 4 + i) * 64] = f32x4{acc[r][4 * i], acc[r][4 * i + 1], acc[r][4 * i + 2], acc[r][4 * i + 3]};
    pos = end;
    first = false;
  }
}

}

namespace fast {
#define PF 32
constexpr int ANT = 640;
constexpr int BNT = 512;
constexpr int SEGW = 128;
constexpr int LCAP = 8 * SEGW + 48;
constexpr int TROW = 528;
constexpr int MAXE = 256 * ANT;
constexpr size_t W_SLOT = 0;
constexpr size_t W_OVN = W_SLOT + 256 * 256 * 64;
constexpr size_t W_WR = W_OVN + 1024;
constexpr size_t W_WO = W_WR + 8192;
constexpr size_t W_OVF = W_WO + 131072;
constexpr size_t W_END = W_OVF + (size_t)MAXE * 8;
constexpr u32 INV = 0xffffffffu;
constexpr int O_CNT = 0;
constexpr int O_BASE = O_CNT + 1024;
constexpr int O_DEG = O_BASE + 1024;
constexpr int O_RANK = O_DEG + 256;
constexpr int O_INV = O_RANK + 256;
constexpr int O_ZERO = O_INV + 256;
constexpr int O_MISC = O_ZERO + 64;
constexpr int O_WRF = O_MISC + 64;
constexpr int O_EXP = O_WRF + 8192;
constexpr int O_TMP = O_EXP + 8 * SEGW * 8;
constexpr int O_META = O_TMP + LCAP * 4;
constexpr int O_AT = O_META + LCAP * 4;
constexpr int PCAP = 8 * SEGW;
constexpr int O_EA = O_AT + PCAP * 32;
constexpr int O_TILE = O_AT;
constexpr int LDS_BYTES = O_TILE + 160 * TROW;
static_assert(O_EA + PCAP * 32 + 32 <= LDS_BYTES, "LDS");
static_assert(LDS_BYTES <= 163840, "LDS");
static_assert((O_AT % 16) == 0 && (O_EA % 16) == 0 && (O_TILE % 16) == 0 && (O_META % 16) == 0, "align");

#define DPPADD(t, ctrl, rmask, bc) t += (u32)__builtin_amdgcn_update_dpp(0, (int)(t), ctrl, rmask, 0xf, bc)
DI u32 scan16(u32 t) {
  DPPADD(t, 0x111, 0xf, true);
  DPPADD(t, 0x112, 0xf, true);
  DPPADD(t, 0x114, 0xf, true);
  DPPADD(t, 0x118, 0xf, true);
  return t;
}
DI u32 scan32(u32 t) { t = scan16(t); DPPADD(t, 0x142, 0xa, false); return t; }
DI u32 scan64(u32 t) { t = scan32(t); DPPADD(t, 0x143, 0xc, false); return t; }
DI void wavesync() { __builtin_amdgcn_fence(__ATOMIC_RELEASE, "wavefront"); __builtin_amdgcn_wave_barrier(); __builtin_amdgcn_fence(__ATOMIC_ACQUIRE, "wavefront"); }

__global__ __launch_bounds__(ANT) void bin_kernel(
    const int* __restrict__ src, const int* __restrict__ dst, const unsigned short* __restrict__ w_r,
    const unsigned short* __restrict__ w_out, char* __restrict__ ws, const char* __restrict__ x,
    const char* __restrict__ sh, const char* __restrict__ eattr, const char* __restrict__ cgp, int N, int E, int S,
    float invN) {
  __shared__ __attribute__((aligned(16))) u32 s_cnt[260];
  __shared__ __attribute__((aligned(16))) u32x4 s_img[1024];
  const int tid0 = threadIdx.x, tid = tid0, lane = tid & 63;
  const int wv = __builtin_amdgcn_readfirstlane(tid >> 6);
  const int i = blockIdx.x;
  ST(0);
  const int e = i * S + tid;
  const bool act = tid < S && e < E;
  const int ec = e < E ? e : E - 1;
  u32 dn = (u32)dst[ec], sn = (u32)src[ec];
  if (tid < 260) s_cnt[tid] = 0u;
  s_img[tid] = u32x4{INV, INV, INV, INV};
  if (tid < 1024 - ANT) s_img[tid + ANT] = u32x4{INV, INV, INV, INV};
  if (wv == 9 && lane < 34) {
    const unsigned short* p;
    char* o;
    if (lane < 32) {
      const int id = i * 32 + lane;
      const int l2 = id & 63, h = (id >> 6) & 1, ks = (id >> 7) & 7, w8 = id >> 10;
      const int q4 = l2 >> 4, c16 = l2 & 15;
      p = w_out + (128 * (q4 & 1) + 64 * (q4 >> 1) + 8 * ks) * 256 + w8 * 32 + 2 * c16 + h;
      o = ws + W_WO + (size_t)id * 16;
    } else {
      const int id = i * 2 + (lane - 32);
      const int l2 = id & 63, r = id >> 6;
      const int kq = l2 >> 4, j = l2 & 15;
      p = w_r + (8 * (kq & 1)) * 256 + 8 * (16 * (kq >> 1) + j) + r;
      o = ws + W_WR + (size_t)id * 16;
    }
    u32 v[8];
#pragma unroll
    for (int k = 0; k < 8; ++k) v[k] = p[k * 256];
    *(u32x4*)o = u32x4{v[0] | (v[1] << 16), v[2] | (v[3] << 16), v[4] | (v[5] << 16), v[6] | (v[7] << 16)};
  }
  u32 pf0 = 0, pf1 = 0, pf2 = 0;
  if (PF & 2) {
    pf1 = *(const u32*)(eattr + (size_t)ec * 32);
    pf2 = *(const u32*)(sh + (size_t)ec * 8);
  }
  if (PF & 1) {
    const u32 g = umin((u32)(i * ANT + tid), (u32)N * ((PF & 16) ? 8u : 16u) - 1u);
    pf0 = *(const u32*)(x + (size_t)g * 128);
  }
  if (PF & 32) {
    u64 pc;
    asm volatile(
        "s_getpc_b64 s[40:41]\n\t"
        "s_add_u32 s40, s40, _ZN4fast10agg_kernelEPKcS1_S1_PKjPcS4_iif@rel32@lo+4\n\t"
        "s_addc_u32 s41, s41, _ZN4fast10agg_kernelEPKcS1_S1_PKjPcS4_iif@rel32@hi+12\n\t"
        "s_mov_b64 %0, s[40:41]"
        : "=s"(pc)
        :
        : "s40", "s41", "scc");
    const char* a = (const char*)pc + umin((u32)tid, 111u) * 128u;
    if ((PF & 64) && tid >= 112) a = cgp;
    pf0 = *(const __attribute__((address_space(1))) u32*)a;
  }
  __syncthreads();
  asm volatile("" : "+v"(dn), "+v"(sn));
  u32 b = (u32)((float)(dn * 256u + 255u) * invN);
  b = umin(b, 255u);
  if ((((b + 1u) * (u32)N) >> 8) <= dn) b += 1u;
  if (((b * (u32)N) >> 8) > dn) b -= 1u;
  b = umin(b, 255u);
  const u32 loc = dn - ((b * (u32)N) >> 8);
  if (act) {
    const u32 rec = (sn << 16) | ((u32)tid << 6) | loc;
    const u32 rk = atomicAdd(&s_cnt[b], 1u);
    if (rk < 16u) {
      ((u32*)s_img)[b * 16u + rk] = rec;
    } else {
      const u32 o = atomicAdd(&s_cnt[256], 1u);
      ((u32x2*)(ws + W_OVF))[i * ANT + o] = u32x2{rec, b};
    }
  }
  __syncthreads();
  u32x4* po = (u32x4*)(ws + W_SLOT) + i * 4;
  po[(tid >> 2) * 1024 + (tid & 3)] = s_img[tid];
  if (tid < 1024 - ANT) po[((tid + ANT) >> 2) * 1024 + (tid & 3)] = s_img[tid + ANT];
  if (tid == ANT - 1) ((u32*)(ws + W_OVN))[i] = s_cnt[256];
  if (PF & 35) asm volatile("" ::"v"(pf0), "v"(pf1), "v"(pf2));
  ST(1);
}

struct Ctx {
  const char* xb;
  u32 xoff;
  u32 js;
  u32 atl;
  u32 mad;
  u32 ead, east;
  u32 eao;
  u32 wra;
};

DI void load_x(const Ctx& c, u32x2 m, u32x4 (&X)[8]) {
#pragma unroll
  for (int i = 0; i < 2; ++i) {
    const char* p = c.xb + (size_t)(((m[i] >> 18) << 11) | c.xoff);
#pragma unroll
    for (int u = 0; u < 4; ++u) X[4 * i + u] = *(const u32x4*)(p + u * 512);
  }
}

DI f32x4 rwm(char* smem, const Ctx& c, const u32x4& eaf, int r) {
  const u32x4 w = *(const u32x4*)(smem + c.wra + r * 1024);
  const f32x4 z4 = {0.f, 0.f, 0.f, 0.f};
  return MFMA16(__builtin_bit_cast(bf16x8, eaf), __builtin_bit_cast(bf16x8, w), z4);
}

template <int PH>
DI void step(char* smem, const Ctx& c, int g, u32x2& m0, u32x2& m1, u32x4& eaf, f32x4 (&rw)[8],
             const u32x4 (&X)[8], u32x4 (&Y)[8], f32x16 (&acc)[8]) {
  u32x4 bt;
  u32 me = 0;
#pragma unroll
  for (int i = 0; i < 2; ++i) {
    const u32 a = ((m0[i] & 15u) == c.js) ? c.atl + (m0[i] & 0x7ff0u) * 2u : (u32)O_ZERO;
    const u32x2 t = *(const u32x2*)(smem + a);
    bt[2 * i] = t.x;
    bt[2 * i + 1] = t.y;
  }
  if (PH != 2) {
    asm volatile("" ::"v"(X[0]), "v"(X[1]), "v"(X[2]), "v"(X[3]), "v"(X[4]), "v"(X[5]), "v"(X[6]), "v"(X[7]));
    load_x(c, m1, Y);
    m0 = m1;
    m1 = *(const u32x2*)(smem + c.mad + (g + 2) * 16);
    __builtin_amdgcn_sched_barrier(0);
  }
#pragma unroll
  for (int r = 0; r < 8; ++r) {
    if (PH == 0 && r < 6) rw[r + 2] = rwm(smem, c, eaf, r + 2);
    if (PH == 1 && r == 2) me = *(const u32*)(smem + c.ead + ((g + 1) >> 1) * c.east);
    if (PH == 1 && r == 4) eaf = *(const u32x4*)(smem + c.eao + (me & 0x7ff0u) * 2u);
    u32x4 z;
#pragma unroll
    for (int i = 0; i < 2; ++i) {
      const float s = rw[r][(PH == 1 ? 2 : 0) + i];
#pragma unroll
      for (int up = 0; up < 2; ++up) {
        const u32 a = X[4 * i + 2 * up][r >> 1], b = X[4 * i + 2 * up + 1][r >> 1];
        z[2 * i + up] = (r & 1) ? cvtpk(bfhi(a) * s, bfhi(b) * s) : cvtpk(bflo(a) * s, bflo(b) * s);
      }
    }
    acc[r] = MFMA32(__builtin_bit_cast(bf16x8, z), __builtin_bit_cast(bf16x8, bt), acc[r]);
    if (PH == 1 && r >= 6) rw[r - 6] = rwm(smem, c, eaf, r - 6);
  }
}

#define STAMP_LDS 0
#define STA(k) do { } while (0)
#define STDUMP() do { } while (0)
__global__ __launch_bounds__(BNT, 2) void agg_kernel(const char* __restrict__ x, const char* __restrict__ sh,
                                                     const char* __restrict__ eattr, const u32* __restrict__ cg2,
                                                     char* __restrict__ out, char* __restrict__ ws, int N, int S,
                                                     float bscale) {
  __shared__ __attribute__((aligned(16))) char smem[LDS_BYTES + STAMP_LDS];
  const int tid0 = threadIdx.x;
  const int wv = __builtin_amdgcn_readfirstlane(tid0 >> 6);
  const int b = blockIdx.x;
  const u32 n0 = (u32)(b * N) >> 8;
  const u32 cntN = ((u32)((b + 1) * N) >> 8) - n0;
  u32* s_cnt = (u32*)(smem + O_CNT);
  u32* s_base = (u32*)(smem + O_BASE);
  u32* s_deg = (u32*)(smem + O_DEG);
  u32* s_rank = (u32*)(smem + O_RANK);
  u32* s_inv = (u32*)(smem + O_INV);
  u32* s_misc = (u32*)(smem + O_MISC);
  u32* s_meta = (u32*)(smem + O_META);
  u32x2* s_exp = (u32x2*)(smem + O_EXP) + wv * SEGW;
  u32* s_tmp = (u32*)(smem + O_TMP);
  STA(2);

  {
    if (tid0 >= 256 && tid0 < 256 + 64) s_deg[tid0 - 256] = 0u;
    if (tid0 >= 384 && tid0 < 384 + 64) s_inv[tid0 - 384] = 0xffffffffu;
    if (tid0 >= 448 && tid0 < 448 + 32) ((u32*)(smem + O_ZERO))[tid0 - 448] = (tid0 == 448 + 18) ? (u32)PCAP << 4 : 0u;
  }

  f32x16 acc[8];
#pragma unroll
  for (int r = 0; r < 8; ++r) {
#pragma unroll
    for (int k = 0; k < 16; ++k) {
      float z = 0.f;
      asm volatile("" : "+v"(z));
      acc[r][k] = z;
    }
  }

  int nseg = 1, nseg0 = 1, cps = 1;
  for (int seg = 0;; ++seg) {
    int tid = tid0;
    asm volatile("" : "+v"(tid));
    const int lane = tid & 63;
    const int q4 = lane >> 4, c16 = lane & 15;
    u32x4 cd[2];
    u32 eb;
    int sb = seg * SEGW;
    u32 ovn = 0;
    if (seg < nseg0) {
      const u32x4* sp = (const u32x4*)(ws + W_SLOT) + (b * 512 + tid) * 2;
      cd[0] = sp[0];
      cd[1] = sp[1];
      ovn = ((const u32*)(ws + W_OVN))[tid & 255];
      eb = (u32)((tid >> 1) * S);
    } else {
      const int idx = (seg - nseg0) * 8 + wv;
      const int sl = idx / cps, k0 = (idx - sl * cps) * SEGW + lane;
      const int n = (int)((const u32*)(ws + W_OVN))[sl];
      cd[0] = u32x4{INV, INV, INV, INV};
      cd[1] = cd[0];
#pragma unroll
      for (int j = 0; j < 2; ++j)
        if (k0 + j * 64 < n) {
          const u32x2 o = ((const u32x2*)(ws + W_OVF))[sl * ANT + k0 + j * 64];
          if (o.y == (u32)b) cd[0][j] = o.x;
        }
      eb = (u32)(sl * S);
      sb = 0;
    }
    const u32x4 wr0 = *(const u32x4*)(ws + W_WR + tid * 16);
    u32 pf0 = 0, pf1 = 0;
    if ((PF & 4) && seg == 0) {
      const u32 g = (u32)(b * BNT + tid), lim = (u32)N * 16u, g0 = (PF & 16) ? (u32)N * 8u : 0u;
      if (g0 + g < lim) pf0 = *(const u32*)(x + (size_t)(g0 + g) * 128);
      if (!(PF & 16) && g + 256u * BNT < lim) pf1 = *(const u32*)(x + (size_t)(g + 256u * BNT) * 128);
    }
    if ((PF & 8) && seg == 0) {
      if (lane == 0) pf1 = *(const u32*)(out + (size_t)n0 * 2048 + wv * 8192);
    }
    if (tid < 256) s_cnt[tid] = 0u;
    __syncthreads();
    u32 tc = 0;
#pragma unroll
    for (int k = 0; k < 8; ++k) tc += (cd[k >> 2][k & 3] != INV) ? 1u : 0u;
    const u32 tinc = scan64(tc);
    const int T = __builtin_amdgcn_readlane((int)tinc, 63);
    STA(3);
    {
      u32 p = tinc - tc - (u32)sb;
#pragma unroll
      for (int k = 0; k < 8; ++k) {
        const u32 r = cd[k >> 2][k & 3];
        const bool v = r != INV;
        if (k >= 4 && __builtin_amdgcn_ballot_w64(v) == 0ull) break;
        if (v && p < (u32)SEGW) s_exp[p] = u32x2{((r >> 16) << 18) | (eb + ((r >> 6) & 1023u)), r & 63u};
        p += v ? 1u : 0u;
      }
    }
    int Tn = T - sb;
    Tn = Tn < 0 ? 0 : (Tn > SEGW ? SEGW : Tn);
    wavesync();
    u32x2 mt[2], shv[2];
    u32x4 e0[2], e1[2];
    bool act[2];
    u32 eid[2];
#pragma unroll
    for (int j = 0; j < 2; ++j) {
      act[j] = j * 64 + lane < Tn;
      mt[j] = s_exp[j * 64 + lane];
      eid[j] = act[j] ? (mt[j].x & 0x3ffffu) : 0u;
    }
    STA(4);
    if (seg == 0) {
#pragma unroll
      for (int j = 0; j < 2; ++j)
        if (act[j]) atomicAdd(&s_deg[mt[j].y], 1u);
      if (lane == 0) atomicMax(&s_misc[0], (u32)T);
      if (ovn) atomicMax(&s_misc[1], ovn);
    }
    *(u32x4*)(smem + O_WRF + tid * 16) = wr0;
    if (tid < 2) *(u32x4*)(smem + O_EA + PCAP * 32 + tid * 16) = u32x4{0u, 0u, 0u, 0u};
#pragma unroll
    for (int j = 0; j < 2; ++j) shv[j] = *(const u32x2*)(sh + (size_t)eid[j] * 8);
    if (seg == 0) {
      STA(5);
      __syncthreads();
      const u32 d = umin(s_deg[lane], 127u);
      u32 gl = 0u, gh = 0u, el = 0xffffffffu, eh = 0xffu;
#pragma unroll
      for (int bit = 6; bit >= 0; --bit) {
        const bool on = (d >> bit) & 1u;
        const u64 B = __builtin_amdgcn_ballot_w64(on);
        const u32 bl = (u32)B, bh = (u32)(B >> 32);
        gl |= on ? 0u : (el & bl);
        gh |= on ? 0u : (eh & bh);
        el &= on ? bl : ~bl;
        eh &= on ? bh : ~bh;
      }
      const u32 rk = __builtin_popcount(gl) + __builtin_popcount(gh) +
                     __builtin_amdgcn_mbcnt_hi(eh, __builtin_amdgcn_mbcnt_lo(el, 0u));
      s_rank[lane] = rk;
      nseg0 = (int)((__builtin_amdgcn_readfirstlane(s_misc[0]) + SEGW - 1) / SEGW);
      nseg0 = nseg0 < 1 ? 1 : nseg0;
      cps = (int)((__builtin_amdgcn_readfirstlane(s_misc[1]) + SEGW - 1) / SEGW);
      nseg = nseg0 + 32 * cps;
      if (wv == 0 && (u32)lane < cntN) {
        const u32 slot = rk >> 3, r8 = rk & 7u;
        s_inv[((slot & 1u) ? 7u - r8 : r8) * 8u + slot] = (u32)lane;
      }
      wavesync();
    }
    if (PF & 12) asm volatile("" ::"v"(pf0), "v"(pf1));
    STA(6);
    u32 key[2], rk[2], slot[2], comp[2];
#pragma unroll
    for (int j = 0; j < 2; ++j) {
      key[j] = 0u; rk[j] = 0u; slot[j] = 0u; comp[j] = 0u;
      if (j * 64 < Tn) {
        const u32 nr = s_rank[act[j] ? mt[j].y : 0u];
        slot[j] = nr >> 3;
        const u32 r8 = nr & 7u;
        const u32 bk = umin((u32)((float)(mt[j].x >> 18) * bscale), 31u);
        key[j] = ((slot[j] & 1u) ? 7u - r8 : r8) * 32u + bk;
        comp[j] = mt[j].x;
        if (act[j]) rk[j] = atomicAdd(&s_cnt[key[j]], 1u);
      }
    }
    e0[0] = *(const u32x4*)(eattr + (size_t)eid[0] * 32);
    e1[0] = *(const u32x4*)(eattr + (size_t)eid[0] * 32 + 16);
    __syncthreads();
    STA(7);
    u32 ls = 0, lc = 0;
    {
      const u32x4 c = *(const u32x4*)(s_cnt + 4 * lane);
      const u32 p1 = c.x, p2 = p1 + c.y, p3 = p2 + c.z, tot = p3 + c.w;
      const u32 inc = scan64(tot);
      u32 run = 0, prev = 0, adj = 0;
#pragma unroll
      for (int k = 0; k < 8; ++k) {
        const u32 ek = __builtin_amdgcn_readlane(inc, 8 * k + 7);
        const u32 ck = ek - prev;
        ls = (wv == k) ? run : ls;
        lc = (wv == k) ? ck : lc;
        adj = ((lane >> 3) == k) ? run - prev : adj;
        run += ck ? ((ck + 3u) & ~3u) : 4u;
        prev = ek;
      }
      const u32 ex = inc - tot + adj;
      *(u32x4*)(s_base + 4 * lane) = u32x4{ex, ex + p1, ex + p2, ex + p3};
      wavesync();
    }
    u32 kb[2], kn[2];
#pragma unroll
    for (int j = 0; j < 2; ++j) {
      kb[j] = 0u;
      kn[j] = 0u;
      if (j * 64 < Tn) {
        kb[j] = s_base[key[j]];
        kn[j] = act[j] ? s_cnt[key[j]] : 0u;
        if (act[j]) s_tmp[kb[j] + rk[j]] = comp[j];
      }
    }
    e0[1] = *(const u32x4*)(eattr + (size_t)eid[1] * 32);
    e1[1] = *(const u32x4*)(eattr + (size_t)eid[1] * 32 + 16);
    __syncthreads();
    STA(8);
    {
      u32 p[2] = {kb[0], kb[1]};
      const u32 nm = kn[0] > kn[1] ? kn[0] : kn[1];
#pragma clang loop unroll(disable) vectorize(disable) interleave(disable)
      for (u32 k = 0; k < nm; k += 4) {
        u32 t[2][4];
#pragma unroll
        for (int j = 0; j < 2; ++j)
#pragma unroll
          for (int q = 0; q < 4; ++q) t[j][q] = s_tmp[kb[j] + k + q];
#pragma unroll
        for (int j = 0; j < 2; ++j)
#pragma unroll
          for (int q = 0; q < 4; ++q) p[j] += (k + q < kn[j] && t[j][q] < comp[j]) ? 1u : 0u;
      }
#pragma unroll
      for (int j = 0; j < 2; ++j)
        if (act[j]) s_meta[p[j]] = (comp[j] & 0xfffc0000u) | ((u32)(wv * SEGW + j * 64 + lane) << 4) | slot[j];
    }
    const u32 lp = lc ? ((lc + 3u) & ~3u) : 4u;
    if (lane < 4 && lc + lane < lp) {
      const u32 p = ls + lc + lane;
      const u32 sl = lc ? (s_tmp[ls + lc - 1] & 0xfffc0000u) : 0u;
      s_meta[p] = sl | ((u32)PCAP << 4) | 15u;
    }
    __syncthreads();
    STA(9);

    u32x4 wf[8][2];
    {
      const int ng = (int)(lp >> 2);
      const int h = lane >> 5, j = lane & 31;
      Ctx c;
      c.xb = x;
      c.xoff = j * 16;
      c.js = j >> 2;
      c.atl = O_AT + (j & 3) * 8;
      c.mad = O_META + (ls + 2 * h) * 4;
      const bool match = (q4 >> 1) == ((c16 >> 2) & 1);
      c.ead = match ? O_META + (ls + 4 * ((c16 >> 1) & 1) + 2 * (c16 >> 3) + (c16 & 1)) * 4 : O_MISC + 8;
      c.east = match ? 32 : 0;
      c.eao = O_EA + 16 * (q4 & 1);
      c.wra = O_WRF + lane * 16;
      u32x2 m0 = *(const u32x2*)(smem + c.mad);
      u32x2 m1 = *(const u32x2*)(smem + c.mad + 16);
      u32x4 XA[8], XB[8];
      load_x(c, m0, XA);
      const u32 me0 = *(const u32*)(smem + c.ead);
      __builtin_amdgcn_sched_barrier(0);
      u32x4 at[2][2];
      u64 cga = (u64)cg2;
      asm volatile("" : "+s"(cga));
      const __attribute__((address_space(4))) u32* cgp = (const __attribute__((address_space(4))) u32*)cga;
#pragma unroll
      for (int j = 0; j < 2; ++j) {
        at[j][0] = u32x4{0u, 0u, 0u, 0u};
        at[j][1] = at[j][0];
        if (j * 64 < Tn) {
          const float s0 = bflo(shv[j].x), s1 = bfhi(shv[j].x), s2 = bflo(shv[j].y), s3 = bfhi(shv[j].y);
#pragma unroll
          for (int w = 0; w < 4; ++w) {
            float a[4];
#pragma unroll
            for (int u = 0; u < 4; ++u) {
              const u32 c0 = cgp[u * 8 + 0 + (w >> 1)], c1 = cgp[u * 8 + 2 + (w >> 1)];
              const u32 c2 = cgp[u * 8 + 4 + (w >> 1)], c3 = cgp[u * 8 + 6 + (w >> 1)];
              a[u] = ((w & 1) ? bfhi(c0) : bflo(c0)) * s0 + ((w & 1) ? bfhi(c1) : bflo(c1)) * s1 +
                     ((w & 1) ? bfhi(c2) : bflo(c2)) * s2 + ((w & 1) ? bfhi(c3) : bflo(c3)) * s3;
            }
            at[j][w >> 1][2 * (w & 1)] = cvtpk(a[0], a[1]);
            at[j][w >> 1][2 * (w & 1) + 1] = cvtpk(a[2], a[3]);
          }
        }
      }
#pragma unroll
      for (int j = 0; j < 2; ++j) {
        if (j * 64 < Tn) {
          const int p = wv * SEGW + j * 64 + lane;
          u32x4* pa = (u32x4*)(smem + O_AT + p * 32);
          u32x4* pe = (u32x4*)(smem + O_EA + p * 32);
          pa[0] = at[j][0];
          pa[1] = at[j][1];
          pe[0] = e0[j];
          pe[1] = e1[j];
        }
      }
      __syncthreads();
      f32x4 rw[8];
      u32x4 eaf = *(const u32x4*)(smem + c.eao + (me0 & 0x7ff0u) * 2u);
#pragma unroll
      for (int r = 2; r < 8; ++r) rw[r] = f32x4{0.f, 0.f, 0.f, 0.f};
      rw[0] = rwm(smem, c, eaf, 0);
      rw[1] = rwm(smem, c, eaf, 1);
      int g = 0;
      for (; g + 2 < ng; g += 2) {
        step<0>(smem, c, g, m0, m1, eaf, rw, XA, XB, acc);
        step<1>(smem, c, g + 1, m0, m1, eaf, rw, XB, XA, acc);
      }
      if (g + 1 < ng) {
        step<0>(smem, c, g, m0, m1, eaf, rw, XA, XB, acc);
        ++g;
#pragma unroll
        for (int k = 0; k < 8; ++k) XA[k] = XB[k];
#pragma unroll
        for (int r = 0; r < 8; ++r) { rw[r][0] = rw[r][2]; rw[r][1] = rw[r][3]; }
      } else {
#pragma unroll
        for (int r = 2; r < 8; ++r) rw[r] = rwm(smem, c, eaf, r);
      }
      step<2>(smem, c, g, m0, m1, eaf, rw, XA, XB, acc);
      __builtin_amdgcn_sched_barrier(0);
      {
        const char* wp = ws + W_WO + (size_t)(wv * 16 * 64 + lane) * 16;
#pragma unroll
        for (int ks = 0; ks < 8; ++ks) {
          wf[ks][0] = *(const u32x4*)(wp + (ks * 2 + 0) * 1024);
          wf[ks][1] = *(const u32x4*)(wp + (ks * 2 + 1) * 1024);
        }
        __builtin_amdgcn_sched_barrier(0);
      }
    }
    STA(10);
    __syncthreads();
    if (seg + 1 < nseg) continue;

    {
      const u32 loc = s_inv[wv * 8 + ((lane & 31) >> 2)];
      if (loc != 0xffffffffu) {
        char* rowp = smem + O_TILE + (loc * 4 + (lane & 3)) * TROW + (lane >> 5) * 64;
#pragma unroll
        for (int a = 0; a < 16; ++a)
          *(u32x4*)(rowp + (a >> 2) * 128 + (a & 3) * 16) =
              u32x4{cvtpk(acc[0][a], acc[1][a]), cvtpk(acc[2][a], acc[3][a]), cvtpk(acc[4][a], acc[5][a]),
                    cvtpk(acc[6][a], acc[7][a])};
      }
    }
    STA(11);
    __syncthreads();
    STA(12);
    {
      const int cb = 128 * (q4 & 1) + 64 * (q4 >> 1);
      const char* tp = smem + O_TILE + c16 * TROW + cb * 2;
      char* op = out + ((size_t)n0 * 4 + c16) * 512 + (wv * 32 + 8 * q4) * 2;
      const u32 nrows = cntN * 4;
#pragma unroll 2
      for (int rb = 0; rb < 10; ++rb) {
        f32x4 a0 = {0.f, 0.f, 0.f, 0.f}, a1 = {0.f, 0.f, 0.f, 0.f};
#pragma unroll
        for (int ks = 0; ks < 8; ++ks) {
          const u32x4 bf = *(const u32x4*)(tp + rb * 16 * TROW + ks * 16);
          a0 = MFMA16(__builtin_bit_cast(bf16x8, wf[ks][0]), __builtin_bit_cast(bf16x8, bf), a0);
          a1 = MFMA16(__builtin_bit_cast(bf16x8, wf[ks][1]), __builtin_bit_cast(bf16x8, bf), a1);
        }
        if ((u32)(rb * 16 + c16) < nrows) {
          const u32x4 o = {cvtpk(a0[0], a1[0]), cvtpk(a0[1], a1[1]), cvtpk(a0[2], a1[2]), cvtpk(a0[3], a1[3])};
          *(u32x4*)(op + (size_t)rb * 16 * 512) = o;
        }
      }
    }
    STA(13);
    STDUMP();
    return;
  }
}
}

extern "C" void kernel_launch(void* const* d_in, const int* in_sizes, int n_in, void* d_out, int out_size,
                              void* d_ws, size_t ws_size, hipStream_t stream) {
  const int N = in_sizes[0] / 1024;
  const int E = in_sizes[1];
  if (N <= 0) return;
  if (N <= 10240 && E >= 1 && E <= fast::MAXE && ws_size >= fast::W_END) {
    int S = (E + 255) / 256;
    if (S < 1) S = 1;
    fast::bin_kernel<<<256, fast::ANT, 0, stream>>>((const int*)d_in[1], (const int*)d_in[2],
                                                    (const unsigned short*)d_in[6], (const unsigned short*)d_in[7],
                                                    (char*)d_ws, (const char*)d_in[0], (const char*)d_in[3],
                                                    (const char*)d_in[4], (const char*)d_in[5], N, E, S, 1.0f / (float)N);
    fast::agg_kernel<<<256, fast::BNT, 0, stream>>>((const char*)d_in[0], (const char*)d_in[3], (const char*)d_in[4],
                                                    (const u32*)d_in[5], (char*)d_out, (char*)d_ws, N, S,
                                                    32.0f / (float)N);
    return;
  }
  int G = (N + gen::MAXN - 1) / gen::MAXN;
  if (G < 256) G = 256;
  if (G > N) G = N;
  size_t gmax = ws_size / ((size_t)gen::NW * 16384);
  if (gmax > 65536) gmax = 65536;
  if (gmax < 1) return;
  for (int b0 = 0; b0 < G; b0 += (int)gmax) {
    const int g = (G - b0 < (int)gmax) ? G - b0 : (int)gmax;
    gen::mace_kernel<<<g, gen::NT, 0, stream>>>(
        (const char*)d_in[0], (const int*)d_in[1], (const int*)d_in[2], (const char*)d_in[3], (const char*)d_in[4],
        (const unsigned short*)d_in[5], (const char*)d_in[6], (const char*)d_in[7], (char*)d_out, (float*)d_ws, N, E,
        b0, G, (float)gen::NB / (float)N);
  }
}
```

```cpp
#include <hip/hip_runtime.h>
#include <hip/hip_bf16.h>
#include <stdint.h>

typedef unsigned int u32;
typedef unsigned long long u64;
typedef __attribute__((ext_vector_type(8))) short bf16x8;
typedef __attribute__((ext_vector_type(2))) __bf16 bf16x2_t;
typedef __attribute__((ext_vector_type(2))) float f32x2;
typedef __attribute__((ext_vector_type(4))) float f32x4;
typedef __attribute__((ext_vector_type(16))) float f32x16;
typedef __attribute__((ext_vector_type(2))) unsigned int u32x2;
typedef __attribute__((ext_vector_type(4))) unsigned int u32x4;

#define DI __device__ __forceinline__
#define ST(k) do { } while (0)
#define MFMA32(a, b, c) __builtin_amdgcn_mfma_f32_32x32x16_bf16((a), (b), (c), 0, 0, 0)
#define MFMA16(a, b, c) __builtin_amdgcn_mfma_f32_16x16x32_bf16((a), (b), (c), 0, 0, 0)

DI u32 cvtpk(float a, float b) {
  f32x2 v = {a, b};
  bf16x2_t r = __builtin_convertvector(v, bf16x2_t);
  return __builtin_bit_cast(u32, r);
}
DI float bflo(u32 v) { return __builtin_bit_cast(float, v << 16); }
DI float bfhi(u32 v) { return __builtin_bit_cast(float, v & 0xffff0000u); }
DI u32 mbcnt(u64 m) { return __builtin_amdgcn_mbcnt_hi((u32)(m >> 32), __builtin_amdgcn_mbcnt_lo((u32)m, 0u)); }
DI u32 umin(u32 a, u32 b) { return a < b ? a : b; }

namespace gen {
constexpr int NT = 768, NW = 12, NRG = 6, NB = 32;
constexpr int MAXN = 40;
constexpr int CAP = 944;
constexpr int LCAP = CAP + 48;
constexpr int CAPW = 128;
constexpr int WMIN = 512;
constexpr int SU = 9;
constexpr int TROW = 528;
constexpr int PFROWS = 1536;

constexpr int O_CNT = 0;
constexpr int O_BASE = O_CNT + 192 * 4;
constexpr int O_DEG = O_BASE + 192 * 4;
constexpr int O_MAP = O_DEG + 64 * 4;
constexpr int O_INV = O_MAP + 64 * 4;
constexpr int O_CGT = O_INV + 64 * 4;
constexpr int O_ZERO = O_CGT + 64 * 4;
constexpr int O_MISC = O_ZERO + 64;
constexpr int O_WR = O_MISC + 64;
constexpr int O_META = O_WR + 8192;
constexpr int O_AT = O_META + LCAP * 4;
constexpr int O_EA = O_AT + LCAP * 32;
constexpr int O_TILE = O_EA + LCAP * 32;
constexpr int O_CANDQ = O_TILE;
constexpr int O_CANDI = O_CANDQ + NW * CAPW * 16;
constexpr int O_HITE = O_CANDI + NW * CAPW * 4;
constexpr int O_HITL = O_HITE + 1024 * 4;
constexpr int O_TMP = O_HITL + 1024 * 4;
constexpr int LDS_BYTES = O_TILE + MAXN * 4 * TROW;
static_assert(O_TMP + LCAP * 8 <= LDS_BYTES, "alias region too small");
static_assert(LDS_BYTES <= 163840, "LDS");
static_assert((O_AT % 16) == 0 && (O_EA % 16) == 0 && (O_TILE % 16) == 0 && (O_WR % 16) == 0, "align");

struct MainCtx {
  const char* xb;
  u32 xoff;
  u32 myslot;
  u32 atlane;
  u32 metaaddr;
  u32 eaaddr, eastride;
  u32 wraddr;
};

DI void load_block(const char* xb, u32 xoff, u32 m0, u32 m1, u32x2 (&buf)[8]) {
  const u32 o0 = (m0 & 0xfffff800u) | xoff, o1 = (m1 & 0xfffff800u) | xoff;
  const char* p0 = xb + (size_t)o0;
  const char* p1 = xb + (size_t)o1;
#pragma unroll
  for (int u = 0; u < 4; ++u) buf[u] = *(const u32x2*)(p0 + u * 512);
#pragma unroll
  for (int u = 0; u < 4; ++u) buf[4 + u] = *(const u32x2*)(p1 + u * 512);
}

DI u32x4 make_z(const u32x2 (&buf)[8], int r, float s0, float s1) {
  u32x4 z;
#pragma unroll
  for (int i = 0; i < 2; ++i) {
    const float s = i ? s1 : s0;
#pragma unroll
    for (int up = 0; up < 2; ++up) {
      const u32 a = (r & 2) ? buf[4 * i + 2 * up].y : buf[4 * i + 2 * up].x;
      const u32 b = (r & 2) ? buf[4 * i + 2 * up + 1].y : buf[4 * i + 2 * up + 1].x;
      z[2 * i + up] = (r & 1) ? cvtpk(bfhi(a) * s, bfhi(b) * s) : cvtpk(bflo(a) * s, bflo(b) * s);
    }
  }
  return z;
}

template <bool PF>
DI void step(char* smem, const MainCtx& c, int g, u32x4& mc, u32x4& eaf, u32x2 (&xb0)[8], u32x2 (&xb1)[8],
             f32x16 (&acc)[4]) {
  const u32 ab = c.atlane + g * 256;
  u32x2 bt[4];
#pragma unroll
  for (int k = 0; k < 4; ++k) {
    const u32 a = ((mc[k] & 15u) == c.myslot) ? ab + k * 32 : (u32)O_ZERO;
    bt[k] = *(const u32x2*)(smem + a);
  }
  if (PF) mc = *(const u32x4*)(smem + c.metaaddr + (g + 1) * 32);
  f32x4 rw[4];
  const f32x4 z4 = {0.f, 0.f, 0.f, 0.f};
  u32 wra = c.wraddr;
  asm volatile("" : "+v"(wra));
#pragma unroll
  for (int r = 0; r < 4; ++r) {
    const u32x4 wr = *(const u32x4*)(smem + wra + r * 1024);
    rw[r] = MFMA16(__builtin_bit_cast(bf16x8, eaf), __builtin_bit_cast(bf16x8, wr), z4);
  }
  if (PF) eaf = *(const u32x4*)(smem + c.eaaddr + (g + 1) * c.eastride);
  {
    const u32x4 b0 = {bt[0].x, bt[0].y, bt[1].x, bt[1].y};
#pragma unroll
    for (int r = 0; r < 4; ++r) {
      const u32x4 z = make_z(xb0, r, rw[r][0], rw[r][1]);
      acc[r] = MFMA32(__builtin_bit_cast(bf16x8, z), __builtin_bit_cast(bf16x8, b0), acc[r]);
    }
    if (PF) load_block(c.xb, c.xoff, mc[0], mc[1], xb0);
  }
  {
    const u32x4 b1 = {bt[2].x, bt[2].y, bt[3].x, bt[3].y};
#pragma unroll
    for (int r = 0; r < 4; ++r) {
      const u32x4 z = make_z(xb1, r, rw[r][2], rw[r][3]);
      acc[r] = MFMA32(__builtin_bit_cast(bf16x8, z), __builtin_bit_cast(bf16x8, b1), acc[r]);
    }
    if (PF) load_block(c.xb, c.xoff, mc[2], mc[3], xb1);
  }
}

__global__ __launch_bounds__(NT, 3) void mace_kernel(
    const char* __restrict__ x, const int* __restrict__ src, const int* __restrict__ dst,
    const char* __restrict__ sh, const char* __restrict__ eattr, const unsigned short* __restrict__ cg,
    const char* __restrict__ w_r, const char* __restrict__ w_out, char* __restrict__ out,
    float* __restrict__ ws, int N, int E, int b0, int G, float bscale) {
  __shared__ __attribute__((aligned(16))) char smem[LDS_BYTES];
  const int tid0 = threadIdx.x;
  const int wv = __builtin_amdgcn_readfirstlane(tid0 >> 6);
  const int rg = wv >> 1, hf = wv & 1;
  const int b = b0 + blockIdx.x;
  const u32 n0 = (u32)(((u64)b * (u64)N) / (u64)G);
  const u32 cntN = (u32)((((u64)b + 1) * (u64)N) / (u64)G) - n0;

  u32* s_cnt = (u32*)(smem + O_CNT);
  u32* s_base = (u32*)(smem + O_BASE);
  u32* s_deg = (u32*)(smem + O_DEG);
  u32* s_map = (u32*)(smem + O_MAP);
  u32* s_inv = (u32*)(smem + O_INV);
  float* s_cgt = (float*)(smem + O_CGT);
  u32* s_misc = (u32*)(smem + O_MISC);
  u32* s_hitE = (u32*)(smem + O_HITE);
  u32* s_hitL = (u32*)(smem + O_HITL);
  u64* s_tmp = (u64*)(smem + O_TMP);
  u32* s_meta = (u32*)(smem + O_META);

  if (wv < 2) {
    const int lane = tid0 & 63;
    const int q4 = lane >> 4, c16 = lane & 15;
    u32x4 wr[4];
    u32x2 L[8];
    const char* p = w_r + (size_t)(8 * (q4 & 1)) * 512 + (hf * 128 + 64 * (q4 >> 1) + 4 * c16) * 2;
#pragma unroll
    for (int j = 0; j < 8; ++j) L[j] = *(const u32x2*)(p + j * 512);
#pragma unroll
    for (int jj = 0; jj < 4; ++jj) {
      wr[0][jj] = __builtin_amdgcn_perm(L[2 * jj + 1].x, L[2 * jj].x, 0x05040100u);
      wr[1][jj] = __builtin_amdgcn_perm(L[2 * jj + 1].x, L[2 * jj].x, 0x07060302u);
      wr[2][jj] = __builtin_amdgcn_perm(L[2 * jj + 1].y, L[2 * jj].y, 0x05040100u);
      wr[3][jj] = __builtin_amdgcn_perm(L[2 * jj + 1].y, L[2 * jj].y, 0x07060302u);
    }
#pragma unroll
    for (int r = 0; r < 4; ++r) *(u32x4*)(smem + O_WR + hf * 4096 + r * 1024 + lane * 16) = wr[r];
  }
  if (wv == 2) {
    const int tid = tid0 & 63;
    const int w = tid >> 4, u = (tid >> 2) & 3, v = tid & 3;
    s_cgt[tid] = bflo((u32)cg[u * 16 + v * 4 + w]);
    s_map[tid] = (u32)(tid % NRG) | ((u32)(tid / NRG) << 8);
    const u32 l = (u32)((tid & 7) * NRG + (tid >> 3));
    s_inv[tid] = (tid < NRG * 8 && l < cntN) ? l : 0xffffffffu;
    if (tid < 16) ((u32*)(smem + O_ZERO))[tid] = 0u;
  }

  bool first = true, again = false;
  int pos = 0;
  int W = (E + 3) & ~3;
  for (;;) {
    if (again) __syncthreads();
    again = true;
    int tid = tid0;
    asm volatile("" : "+v"(tid));
    const int lane = tid & 63;
    const int q4 = lane >> 4, c16 = lane & 15, h = lane >> 5;
    u32 pfv = 0;
    {
      const u32 line = (u32)(b >> 3) * NT + tid;
      if (first && line < (u32)PFROWS * 16u && line < (u32)N * 16u) pfv = *(const u32*)(x + (size_t)line * 128);
    }
    if (tid < 192) s_cnt[tid] = 0u;
    if (tid >= 192 && tid < 256) s_deg[tid - 192] = 0u;
    if (tid >= 256 && tid < 258) s_misc[tid - 256] = 0u;
    __syncthreads();
    const int end = (E - pos < W) ? E : pos + W;

    {
      const u32x4* dq = (const u32x4*)dst;
      u32x4* cq = (u32x4*)(smem + O_CANDQ) + wv * CAPW;
      u32* ci = (u32*)(smem + O_CANDI) + wv * CAPW;
      const int q0 = pos >> 2, qf = end >> 2;
      u32 nc = 0;
      for (int qb = q0 + wv * 64; qb < qf; qb += NT * SU) {
        u32x4 v[SU];
#pragma unroll
        for (int k = 0; k < SU; ++k) {
          const int q = qb + k * NT + lane;
          v[k] = dq[q < qf ? q : qf - 1];
        }
#pragma unroll
        for (int k = 0; k < SU; ++k) {
          const int q = qb + k * NT + lane;
          const u32 m = umin(umin(v[k].x - n0, v[k].y - n0), umin(v[k].z - n0, v[k].w - n0));
          const bool hit = (m < cntN) && (q < qf);
          const u64 mask = __ballot(hit);
          if (mask) {
            const u32 p = nc + mbcnt(mask);
            if (hit && p < (u32)CAPW) { cq[p] = v[k]; ci[p] = (u32)q; }
            nc += (u32)__popcll(mask);
          }
        }
      }
      if (wv == NW - 1 && (end & 3)) {
        u32x4 v;
        const int e4 = qf * 4;
        v.x = (u32)dst[e4];
        v.y = (e4 + 1 < end) ? (u32)dst[e4 + 1] : n0 - 1u;
        v.z = (e4 + 2 < end) ? (u32)dst[e4 + 2] : n0 - 1u;
        v.w = n0 - 1u;
        if (lane == 0 && nc < (u32)CAPW) { cq[nc] = v; ci[nc] = (u32)qf; }
        nc += 1;
      }
      if (nc > (u32)CAPW) { if (lane == 0) s_misc[1] = 1u; nc = CAPW; }
      __builtin_amdgcn_wave_barrier();
      for (u32 i0 = 0; i0 < nc; i0 += 64) {
        const u32 i = i0 + lane;
        const bool valid = i < nc;
        const u32x4 v = cq[valid ? i : 0];
        const u32 q = ci[valid ? i : 0];
        const u32 t[4] = {v.x - n0, v.y - n0, v.z - n0, v.w - n0};
        u64 mk[4];
        u32 tot = 0;
#pragma unroll
        for (int k = 0; k < 4; ++k) { mk[k] = __ballot(valid && t[k] < cntN); tot += (u32)__popcll(mk[k]); }
        u32 bs = 0;
        if (lane == 0) bs = atomicAdd(&s_misc[0], tot);
        bs = __builtin_amdgcn_readfirstlane(bs);
#pragma unroll
        for (int k = 0; k < 4; ++k) {
          const u32 p = bs + mbcnt(mk[k]);
          if (valid && t[k] < cntN && p < (u32)CAP) {
            s_hitE[p] = 4 * q + k;
            s_hitL[p] = t[k];
            atomicAdd(&s_deg[t[k]], 1u);
          }
          bs += (u32)__popcll(mk[k]);
        }
      }
    }
    asm volatile("" ::"v"(pfv));
    __syncthreads();
    const u32 nh = __builtin_amdgcn_readfirstlane(s_misc[0]);
    if (nh > (u32)CAP || __builtin_amdgcn_readfirstlane(s_misc[1]) != 0u) {
      W = ((W >> 1) + 3) & ~3;
      if (W < WMIN) W = WMIN;
      continue;
    }

    u32 he[2], hl[2], hs[2];
    u32x2 hsh[2];
    u32x4 hea[2][2];
    bool act[2];
#pragma unroll
    for (int j = 0; j < 2; ++j) {
      const u32 i = tid + j * NT;
      act[j] = i < nh;
      he[j] = 0u; hl[j] = 0u; hs[j] = 0u;
      hsh[j] = u32x2{0u, 0u};
      hea[j][0] = u32x4{0u, 0u, 0u, 0u};
      hea[j][1] = u32x4{0u, 0u, 0u, 0u};
      if (j == 0 ? nh > 0u : nh > (u32)NT) {
        const u32 ii = act[j] ? i : 0;
        he[j] = s_hitE[ii];
        hl[j] = s_hitL[ii];
        hs[j] = (u32)src[he[j]];
        hsh[j] = *(const u32x2*)(sh + (size_t)he[j] * 8);
        hea[j][0] = *(const u32x4*)(eattr + (size_t)he[j] * 32);
        hea[j][1] = *(const u32x4*)(eattr + (size_t)he[j] * 32 + 16);
      }
    }
    if (first && wv == NW - 1) {
      const u32 d = s_deg[lane];
      u32 rank = 0;
      for (u32 j = 0; j < cntN; ++j) {
        const u32 dj = __builtin_amdgcn_readlane(d, j);
        rank += (dj > d || (dj == d && j < (u32)lane)) ? 1u : 0u;
      }
      const u32 slot = rank / NRG, r = rank % NRG;
      const u32 g = (slot & 1) ? (NRG - 1 - r) : r;
      s_inv[lane] = 0xffffffffu;
      __builtin_amdgcn_wave_barrier();
      if ((u32)lane < cntN) { s_map[lane] = g | (slot << 8); s_inv[g * 8 + slot] = lane; }
    }
    __syncthreads();

    u32 key[2], rk[2], mp[2];
#pragma unroll
    for (int j = 0; j < 2; ++j) {
      key[j] = 0u; rk[j] = 0u; mp[j] = 0u;
      if (j == 0 ? nh > 0u : nh > (u32)NT) {
        mp[j] = s_map[hl[j]];
        u32 bk = (u32)((float)hs[j] * bscale);
        bk = umin(bk, NB - 1);
        key[j] = (mp[j] & 255u) * NB + bk;
        if (act[j]) rk[j] = atomicAdd(&s_cnt[key[j]], 1u);
      }
    }
    __syncthreads();
    u32 ls = 0, lc = 0;
    {
      u32 v[3], inc[3];
#pragma unroll
      for (int k = 0; k < 3; ++k) {
        v[k] = s_cnt[k * 64 + lane];
        u32 t = v[k];
        t += (u32)__builtin_amdgcn_update_dpp(0, (int)t, 0x111, 0xf, 0xf, true);
        t += (u32)__builtin_amdgcn_update_dpp(0, (int)t, 0x112, 0xf, 0xf, true);
        t += (u32)__builtin_amdgcn_update_dpp(0, (int)t, 0x114, 0xf, 0xf, true);
        t += (u32)__builtin_amdgcn_update_dpp(0, (int)t, 0x118, 0xf, 0xf, true);
        t += (u32)__builtin_amdgcn_update_dpp(0, (int)t, 0x142, 0xa, 0xf, false);
        inc[k] = t;
      }
      u32 run = 0;
#pragma unroll
      for (int k = 0; k < 3; ++k) {
        const u32 c0 = __builtin_amdgcn_readlane(inc[k], 31);
        const u32 c1 = __builtin_amdgcn_readlane(inc[k], 63);
        const u32 s0 = run, s1 = run + ((c0 + 7u) & ~7u);
        run = s1 + ((c1 + 7u) & ~7u);
        ls = (rg == 2 * k) ? s0 : ls;
        lc = (rg == 2 * k) ? c0 : lc;
        ls = (rg == 2 * k + 1) ? s1 : ls;
        lc = (rg == 2 * k + 1) ? c1 : lc;
        s_base[k * 64 + lane] = (h ? s1 : s0) + inc[k] - v[k];
      }
      __builtin_amdgcn_wave_barrier();
    }
    u32 kb[2];
    u64 comp[2];
#pragma unroll
    for (int j = 0; j < 2; ++j) {
      kb[j] = 0u; comp[j] = 0ull;
      if (j == 0 ? nh > 0u : nh > (u32)NT) {
        kb[j] = s_base[key[j]];
        comp[j] = ((u64)hs[j] << 32) | he[j];
        if (act[j]) s_tmp[kb[j] + rk[j]] = comp[j];
      }
    }
    __syncthreads();
#pragma unroll
    for (int j = 0; j < 2; ++j) {
      if ((j == 0 || nh > (u32)NT) && act[j]) {
        const u32 n = s_cnt[key[j]];
        u32 p = kb[j];
        for (u32 k = 0; k < n; ++k) p += (s_tmp[kb[j] + k] < comp[j]) ? 1u : 0u;
        s_meta[p] = (hs[j] << 11) | (mp[j] >> 8);
        const float s0 = bflo(hsh[j].x), s1 = bfhi(hsh[j].x), s2 = bflo(hsh[j].y), s3 = bfhi(hsh[j].y);
        u32 at[8];
#pragma unroll
        for (int w = 0; w < 4; ++w) {
          float a[4];
#pragma unroll
          for (int u = 0; u < 4; ++u) {
            const f32x4 cv = *(const f32x4*)(s_cgt + w * 16 + u * 4);
            a[u] = cv.x * s0 + cv.y * s1 + cv.z * s2 + cv.w * s3;
          }
          at[2 * w] = cvtpk(a[0], a[1]);
          at[2 * w + 1] = cvtpk(a[2], a[3]);
        }
        u32x4* pa = (u32x4*)(smem + O_AT + p * 32);
        pa[0] = u32x4{at[0], at[1], at[2], at[3]};
        pa[1] = u32x4{at[4], at[5], at[6], at[7]};
        u32x4* pe = (u32x4*)(smem + O_EA + p * 32);
        pe[0] = hea[j][0];
        pe[1] = hea[j][1];
      }
    }
    if (hf == 0 && lc > 0) {
      const u32 p = ls + lc + lane;
      if (p < ls + ((lc + 7u) & ~7u)) {
        const u32 sl = (u32)(s_tmp[ls + lc - 1] >> 32);
        s_meta[p] = (sl << 11) | 15u;
        const u32x4 zz = {0u, 0u, 0u, 0u};
        u32x4* pe = (u32x4*)(smem + O_EA + p * 32);
        pe[0] = zz;
        pe[1] = zz;
      }
    }
    __syncthreads();

    f32x16 acc[4];
    if (first) {
#pragma unroll
      for (int r = 0; r < 4; ++r)
#pragma unroll
        for (int i = 0; i < 16; ++i) acc[r][i] = 0.f;
    } else {
      const f32x4* wsp = (const f32x4*)ws + ((size_t)blockIdx.x * NW + wv) * 1024 + lane;
#pragma unroll
      for (int r = 0; r < 4; ++r)
#pragma unroll
        for (int i = 0; i < 4; ++i) {
          const f32x4 t = wsp[(r * 4 + i) * 64];
          acc[r][4 * i] = t.x; acc[r][4 * i + 1] = t.y; acc[r][4 * i + 2] = t.z; acc[r][4 * i + 3] = t.w;
        }
    }
    const int ng = (int)((lc + 7u) >> 3);
    if (ng > 0) {
      MainCtx c;
      c.xb = x;
      c.xoff = hf * 256 + (lane & 31) * 8;
      c.myslot = (lane & 31) >> 2;
      c.atlane = O_AT + (lane & 3) * 8 + (ls + 4 * h) * 32;
      c.metaaddr = O_META + (ls + 4 * h) * 4;
      const bool match = (q4 >> 1) == ((c16 >> 2) & 1);
      c.eaaddr = match ? O_EA + (ls + 4 * (c16 >> 3) + (c16 & 3)) * 32 + 16 * (q4 & 1) : O_ZERO;
      c.eastride = match ? 256 : 0;
      c.wraddr = O_WR + hf * 4096 + lane * 16;
      u32x4 mc = *(const u32x4*)(smem + c.metaaddr);
      u32x4 eaf = *(const u32x4*)(smem + c.eaaddr);
      u32x2 xb0[8], xb1[8];
      load_block(c.xb, c.xoff, mc[0], mc[1], xb0);
      load_block(c.xb, c.xoff, mc[2], mc[3], xb1);
      int g = 0;
      for (; g < ng - 1; ++g) step<true>(smem, c, g, mc, eaf, xb0, xb1, acc);
      step<false>(smem, c, g, mc, eaf, xb0, xb1, acc);
    }
    if (end >= E) {

  u32x4 wf[8][2];
#pragma unroll
  for (int ks = 0; ks < 8; ++ks) { wf[ks][0] = u32x4{0u, 0u, 0u, 0u}; wf[ks][1] = u32x4{0u, 0u, 0u, 0u}; }
  if (wv < 8) {
    const int cb = 128 * (q4 & 1) + 64 * (q4 >> 1);
    const char* p = w_out + (size_t)cb * 512 + (wv * 32 + 2 * c16) * 2;
#pragma unroll
    for (int ks = 0; ks < 8; ++ks) {
      u32 L[8];
#pragma unroll
      for (int j = 0; j < 8; ++j) L[j] = *(const u32*)(p + (ks * 8 + j) * 512);
#pragma unroll
      for (int jj = 0; jj < 4; ++jj) {
        wf[ks][0][jj] = __builtin_amdgcn_perm(L[2 * jj + 1], L[2 * jj], 0x05040100u);
        wf[ks][1][jj] = __builtin_amdgcn_perm(L[2 * jj + 1], L[2 * jj], 0x07060302u);
      }
    }
  }
  {
    const u32 loc = s_inv[rg * 8 + ((lane & 31) >> 2)];
    if (loc != 0xffffffffu) {
      char* rowp = smem + O_TILE + (loc * 4 + (lane & 3)) * TROW + (hf * 128 + 16 * h) * 2;
#pragma unroll
      for (int qq = 0; qq < 4; ++qq) {
        u32 d[8];
#pragma unroll
        for (int a = 0; a < 4; ++a) {
          d[2 * a] = cvtpk(acc[0][4 * qq + a], acc[1][4 * qq + a]);
          d[2 * a + 1] = cvtpk(acc[2][4 * qq + a], acc[3][4 * qq + a]);
        }
        u32x4* pp = (u32x4*)(rowp + qq * 64);
        pp[0] = u32x4{d[0], d[1], d[2], d[3]};
        pp[1] = u32x4{d[4], d[5], d[6], d[7]};
      }
    }
  }
  __syncthreads();
  if (wv < 8) {
    const int cb = 128 * (q4 & 1) + 64 * (q4 >> 1);
    const char* tp = smem + O_TILE + c16 * TROW + cb * 2;
    char* op = out + ((size_t)n0 * 4 + c16) * 512 + (wv * 32 + 8 * q4) * 2;
    const u32 nrows = cntN * 4;
#pragma unroll 2
    for (int rb = 0; rb < MAXN * 4 / 16; ++rb) {
      f32x4 a0 = {0.f, 0.f, 0.f, 0.f}, a1 = {0.f, 0.f, 0.f, 0.f};
#pragma unroll
      for (int ks = 0; ks < 8; ++ks) {
        const u32x4 bf = *(const u32x4*)(tp + rb * 16 * TROW + ks * 16);
        a0 = MFMA16(__builtin_bit_cast(bf16x8, wf[ks][0]), __builtin_bit_cast(bf16x8, bf), a0);
        a1 = MFMA16(__builtin_bit_cast(bf16x8, wf[ks][1]), __builtin_bit_cast(bf16x8, bf), a1);
      }
      if ((u32)(rb * 16 + c16) < nrows) {
        const u32x4 o = {cvtpk(a0[0], a1[0]), cvtpk(a0[1], a1[1]), cvtpk(a0[2], a1[2]), cvtpk(a0[3], a1[3])};
        *(u32x4*)(op + (size_t)rb * 16 * 512) = o;
      }
    }
  }
  return;
    }
    int ln = tid0 & 63;
    asm volatile("" : "+v"(ln));
    f32x4* wsp = (f32x4*)ws + ((size_t)blockIdx.x * NW + wv) * 1024 + ln;
#pragma unroll
    for (int r = 0; r < 4; ++r)
#pragma unroll
      for (int i = 0; i < 4; ++i)
        wsp[(r * 4 + i) * 64] = f32x4{acc[r][4 * i], acc[r][4 * i + 1], acc[r][4 * i + 2], acc[r][4 * i + 3]};
    pos = end;
    first = false;
  }
}

}

namespace fast {
#define CT 3
constexpr int ANT = 640;
constexpr int BNT = 512;
constexpr int SEGW = 128;
constexpr int LCAP = 8 * SEGW + 48;
constexpr int TROW = 528;
constexpr int MAXE = 256 * ANT;
constexpr size_t W_SLOT = 0;
constexpr size_t W_OVN = W_SLOT + 256 * 256 * 64;
constexpr size_t W_WR = W_OVN + 1024;
constexpr size_t W_WO = W_WR + 8192;
constexpr size_t W_OVF = W_WO + 131072;
constexpr size_t W_END = W_OVF + (size_t)MAXE * 8;
constexpr u32 INV = 0xffffffffu;
constexpr int O_CNT = 0;
constexpr int O_BASE = O_CNT + 1024;
constexpr int O_DEG = O_BASE + 1024;
constexpr int O_RANK = O_DEG + 256;
constexpr int O_INV = O_RANK + 256;
constexpr int O_ZERO = O_INV + 256;
constexpr int O_MISC = O_ZERO + 64;
constexpr int O_WRF = O_MISC + 64;
constexpr int O_EXP = O_WRF + 8192;
constexpr int O_TMP = O_EXP + 8 * SEGW * 8;
constexpr int O_META = O_TMP + LCAP * 4;
constexpr int O_AT = O_META + LCAP * 4;
constexpr int PCAP = 8 * SEGW;
constexpr int O_EA = O_AT + PCAP * 32;
constexpr int O_TILE = O_AT;
constexpr int LDS_BYTES = O_TILE + 160 * TROW;
static_assert(O_EA + PCAP * 32 + 32 <= LDS_BYTES, "LDS");
static_assert(LDS_BYTES <= 163840, "LDS");
static_assert((O_AT % 16) == 0 && (O_EA % 16) == 0 && (O_TILE % 16) == 0 && (O_META % 16) == 0, "align");

#define DPPADD(t, ctrl, rmask, bc) t += (u32)__builtin_amdgcn_update_dpp(0, (int)(t), ctrl, rmask, 0xf, bc)
DI u32 scan16(u32 t) {
  DPPADD(t, 0x111, 0xf, true);
  DPPADD(t, 0x112, 0xf, true);
  DPPADD(t, 0x114, 0xf, true);
  DPPADD(t, 0x118, 0xf, true);
  return t;
}
DI u32 scan32(u32 t) { t = scan16(t); DPPADD(t, 0x142, 0xa, false); return t; }
DI u32 scan64(u32 t) { t = scan32(t); DPPADD(t, 0x143, 0xc, false); return t; }
DI void wavesync() { __builtin_amdgcn_fence(__ATOMIC_RELEASE, "wavefront"); __builtin_amdgcn_wave_barrier(); __builtin_amdgcn_fence(__ATOMIC_ACQUIRE, "wavefront"); }

__global__ __launch_bounds__(ANT) void bin_kernel(
    const int* __restrict__ src, const int* __restrict__ dst, const unsigned short* __restrict__ w_r,
    const unsigned short* __restrict__ w_out, char* __restrict__ ws, int N, int E, int S, float invN) {
  __shared__ __attribute__((aligned(16))) u32 s_cnt[260];
  __shared__ __attribute__((aligned(16))) u32x4 s_img[1024];
  const int tid0 = threadIdx.x, tid = tid0, lane = tid & 63;
  const int wv = __builtin_amdgcn_readfirstlane(tid >> 6);
  const int i = blockIdx.x;
  ST(0);
  const int e = i * S + tid;
  const bool act = tid < S && e < E;
  const int ec = e < E ? e : E - 1;
  u32 dn = (u32)dst[ec], sn = (u32)src[ec];
  u32 pf0;
  {
    u64 pc, pa, pk;
    asm volatile(
        "s_getpc_b64 s[40:41]\n\t"
        "s_add_u32 s42, s40, _ZN4fast10agg_kernelEPKcS1_S1_PKjPcS4_iif@rel32@lo+4\n\t"
        "s_addc_u32 s43, s41, _ZN4fast10agg_kernelEPKcS1_S1_PKjPcS4_iif@rel32@hi+12\n\t"
        "s_add_u32 s44, s40, _ZN4fast10agg_kernelEPKcS1_S1_PKjPcS4_iif.kd@rel32@lo+20\n\t"
        "s_addc_u32 s45, s41, _ZN4fast10agg_kernelEPKcS1_S1_PKjPcS4_iif.kd@rel32@hi+28\n\t"
        "s_mov_b64 %0, s[40:41]\n\t"
        "s_mov_b64 %1, s[42:43]\n\t"
        "s_mov_b64 %2, s[44:45]"
        : "=s"(pc), "=s"(pa), "=s"(pk)
        :
        : "s40", "s41", "s42", "s43", "s44", "s45", "scc");
    const char* a = (const char*)pa + umin((u32)tid, 111u) * 128u;
    if ((CT & 2) && tid >= 112 && tid < 121) a = (const char*)pc + (tid - 112) * 128;
    if ((CT & 4) && tid >= 121) a = (const char*)pk;
    pf0 = *(const __attribute__((address_space(1))) u32*)a;
  }
  if (tid < 260) s_cnt[tid] = 0u;
  s_img[tid] = u32x4{INV, INV, INV, INV};
  if (tid < 1024 - ANT) s_img[tid + ANT] = u32x4{INV, INV, INV, INV};
  if (wv == 9 && lane < 34) {
    const unsigned short* p;
    char* o;
    if (lane < 32) {
      const int id = i * 32 + lane;
      const int l2 = id & 63, h = (id >> 6) & 1, ks = (id >> 7) & 7, w8 = id >> 10;
      const int q4 = l2 >> 4, c16 = l2 & 15;
      p = w_out + (128 * (q4 & 1) + 64 * (q4 >> 1) + 8 * ks) * 256 + w8 * 32 + 2 * c16 + h;
      o = ws + W_WO + (size_t)id * 16;
    } else {
      const int id = i * 2 + (lane - 32);
      const int l2 = id & 63, r = id >> 6;
      const int kq = l2 >> 4, j = l2 & 15;
      p = w_r + (8 * (kq & 1)) * 256 + 8 * (16 * (kq >> 1) + j) + r;
      o = ws + W_WR + (size_t)id * 16;
    }
    u32 v[8];
#pragma unroll
    for (int k = 0; k < 8; ++k) v[k] = p[k * 256];
    *(u32x4*)o = u32x4{v[0] | (v[1] << 16), v[2] | (v[3] << 16), v[4] | (v[5] << 16), v[6] | (v[7] << 16)};
  }
  __syncthreads();
  asm volatile("" : "+v"(dn), "+v"(sn));
  u32 b = (u32)((float)(dn * 256u + 255u) * invN);
  b = umin(b, 255u);
  if ((((b + 1u) * (u32)N) >> 8) <= dn) b += 1u;
  if (((b * (u32)N) >> 8) > dn) b -= 1u;
  b = umin(b, 255u);
  const u32 loc = dn - ((b * (u32)N) >> 8);
  if (act) {
    const u32 rec = (sn << 16) | ((u32)tid << 6) | loc;
    const u32 rk = atomicAdd(&s_cnt[b], 1u);
    if (rk < 16u) {
      ((u32*)s_img)[b * 16u + rk] = rec;
    } else {
      const u32 o = atomicAdd(&s_cnt[256], 1u);
      ((u32x2*)(ws + W_OVF))[i * ANT + o] = u32x2{rec, b};
    }
  }
  __syncthreads();
  u32x4* po = (u32x4*)(ws + W_SLOT) + i * 4;
  po[(tid >> 2) * 1024 + (tid & 3)] = s_img[tid];
  if (tid < 1024 - ANT) po[((tid + ANT) >> 2) * 1024 + (tid & 3)] = s_img[tid + ANT];
  if (tid == ANT - 1) ((u32*)(ws + W_OVN))[i] = s_cnt[256];
  asm volatile("" ::"v"(pf0));
  ST(1);
}

struct Ctx {
  const char* xb;
  u32 xoff;
  u32 js;
  u32 atl;
  u32 mad;
  u32 ead, east;
  u32 eao;
  u32 wra;
};

DI void load_x(const Ctx& c, u32x2 m, u32x4 (&X)[8]) {
#pragma unroll
  for (int i = 0; i < 2; ++i) {
    const char* p = c.xb + (size_t)(((m[i] >> 18) << 11) | c.xoff);
#pragma unroll
    for (int u = 0; u < 4; ++u) X[4 * i + u] = *(const u32x4*)(p + u * 512);
  }
}

DI f32x4 rwm(char* smem, const Ctx& c, const u32x4& eaf, int r) {
  const u32x4 w = *(const u32x4*)(smem + c.wra + r * 1024);
  const f32x4 z4 = {0.f, 0.f, 0.f, 0.f};
  return MFMA16(__builtin_bit_cast(bf16x8, eaf), __builtin_bit_cast(bf16x8, w), z4);
}

template <int PH>
DI void step(char* smem, const Ctx& c, int g, u32x2& m0, u32x2& m1, u32x4& eaf, f32x4 (&rw)[8],
             const u32x4 (&X)[8], u32x4 (&Y)[8], f32x16 (&acc)[8]) {
  u32x4 bt;
  u32 me = 0;
#pragma unroll
  for (int i = 0; i < 2; ++i) {
    const u32 a = ((m0[i] & 15u) == c.js) ? c.atl + (m0[i] & 0x7ff0u) * 2u : (u32)O_ZERO;
    const u32x2 t = *(const u32x2*)(smem + a);
    bt[2 * i] = t.x;
    bt[2 * i + 1] = t.y;
  }
  if (PH != 2) {
    asm volatile("" ::"v"(X[0]), "v"(X[1]), "v"(X[2]), "v"(X[3]), "v"(X[4]), "v"(X[5]), "v"(X[6]), "v"(X[7]));
    load_x(c, m1, Y);
    m0 = m1;
    m1 = *(const u32x2*)(smem + c.mad + (g + 2) * 16);
    __builtin_amdgcn_sched_barrier(0);
  }
#pragma unroll
  for (int r = 0; r < 8; ++r) {
    if (PH == 0 && r < 6) rw[r + 2] = rwm(smem, c, eaf, r + 2);
    if (PH == 1 && r == 2) me = *(const u32*)(smem + c.ead + ((g + 1) >> 1) * c.east);
    if (PH == 1 && r == 4) eaf = *(const u32x4*)(smem + c.eao + (me & 0x7ff0u) * 2u);
    u32x4 z;
#pragma unroll
    for (int i = 0; i < 2; ++i) {
      const float s = rw[r][(PH == 1 ? 2 : 0) + i];
#pragma unroll
      for (int up = 0; up < 2; ++up) {
        const u32 a = X[4 * i + 2 * up][r >> 1], b = X[4 * i + 2 * up + 1][r >> 1];
        z[2 * i + up] = (r & 1) ? cvtpk(bfhi(a) * s, bfhi(b) * s) : cvtpk(bflo(a) * s, bflo(b) * s);
      }
    }
    acc[r] = MFMA32(__builtin_bit_cast(bf16x8, z), __builtin_bit_cast(bf16x8, bt), acc[r]);
    if (PH == 1 && r >= 6) rw[r - 6] = rwm(smem, c, eaf, r - 6);
  }
}

#define STAMP_LDS 0
#define STA(k) do { } while (0)
#define STDUMP() do { } while (0)
__global__ __launch_bounds__(BNT, 2) void agg_kernel(const char* __restrict__ x, const char* __restrict__ sh,
                                                     const char* __restrict__ eattr, const u32* __restrict__ cg2,
                                                     char* __restrict__ out, char* __restrict__ ws, int N, int S,
                                                     float bscale) {
  __shared__ __attribute__((aligned(16))) char smem[LDS_BYTES + STAMP_LDS];
  const int tid0 = threadIdx.x;
  const int wv = __builtin_amdgcn_readfirstlane(tid0 >> 6);
  const int b = blockIdx.x;
  const u32 n0 = (u32)(b * N) >> 8;
  const u32 cntN = ((u32)((b + 1) * N) >> 8) - n0;
  u32* s_cnt = (u32*)(smem + O_CNT);
  u32* s_base = (u32*)(smem + O_BASE);
  u32* s_deg = (u32*)(smem + O_DEG);
  u32* s_rank = (u32*)(smem + O_RANK);
  u32* s_inv = (u32*)(smem + O_INV);
  u32* s_misc = (u32*)(smem + O_MISC);
  u32* s_meta = (u32*)(smem + O_META);
  u32x2* s_exp = (u32x2*)(smem + O_EXP) + wv * SEGW;
  u32* s_tmp = (u32*)(smem + O_TMP);
  STA(2);

  {
    if (tid0 >= 256 && tid0 < 256 + 64) s_deg[tid0 - 256] = 0u;
    if (tid0 >= 384 && tid0 < 384 + 64) s_inv[tid0 - 384] = 0xffffffffu;
    if (tid0 >= 448 && tid0 < 448 + 32) ((u32*)(smem + O_ZERO))[tid0 - 448] = (tid0 == 448 + 18) ? (u32)PCAP << 4 : 0u;
  }

  f32x16 acc[8];
#pragma unroll
  for (int r = 0; r < 8; ++r) {
#pragma unroll
    for (int k = 0; k < 16; ++k) {
      float z = 0.f;
      asm volatile("" : "+v"(z));
      acc[r][k] = z;
    }
  }

  int nseg = 1, nseg0 = 1, cps = 1;
  for (int seg = 0;; ++seg) {
    int tid = tid0;
    asm volatile("" : "+v"(tid));
    const int lane = tid & 63;
    const int q4 = lane >> 4, c16 = lane & 15;
    u32x4 cd[2];
    u32 eb;
    int sb = seg * SEGW;
    u32 ovn = 0;
    if (seg < nseg0) {
      const u32x4* sp = (const u32x4*)(ws + W_SLOT) + (b * 512 + tid) * 2;
      cd[0] = sp[0];
      cd[1] = sp[1];
      ovn = ((const u32*)(ws + W_OVN))[tid & 255];
      eb = (u32)((tid >> 1) * S);
    } else {
      const int idx = (seg - nseg0) * 8 + wv;
      const int sl = idx / cps, k0 = (idx - sl * cps) * SEGW + lane;
      const int n = (int)((const u32*)(ws + W_OVN))[sl];
      cd[0] = u32x4{INV, INV, INV, INV};
      cd[1] = cd[0];
#pragma unroll
      for (int j = 0; j < 2; ++j)
        if (k0 + j * 64 < n) {
          const u32x2 o = ((const u32x2*)(ws + W_OVF))[sl * ANT + k0 + j * 64];
          if (o.y == (u32)b) cd[0][j] = o.x;
        }
      eb = (u32)(sl * S);
      sb = 0;
    }
    const u32x4 wr0 = *(const u32x4*)(ws + W_WR + tid * 16);
    if (tid < 256) s_cnt[tid] = 0u;
    __syncthreads();
    u32 tc = 0;
#pragma unroll
    for (int k = 0; k < 8; ++k) tc += (cd[k >> 2][k & 3] != INV) ? 1u : 0u;
    const u32 tinc = scan64(tc);
    const int T = __builtin_amdgcn_readlane((int)tinc, 63);
    STA(3);
    {
      u32 p = tinc - tc - (u32)sb;
#pragma unroll
      for (int k = 0; k < 8; ++k) {
        const u32 r = cd[k >> 2][k & 3];
        const bool v = r != INV;
        if (k >= 4 && __builtin_amdgcn_ballot_w64(v) == 0ull) break;
        if (v && p < (u32)SEGW) s_exp[p] = u32x2{((r >> 16) << 18) | (eb + ((r >> 6) & 1023u)), r & 63u};
        p += v ? 1u : 0u;
      }
    }
    int Tn = T - sb;
    Tn = Tn < 0 ? 0 : (Tn > SEGW ? SEGW : Tn);
    wavesync();
    u32x2 mt[2], shv[2];
    u32x4 e0[2], e1[2];
    bool act[2];
    u32 eid[2];
#pragma unroll
    for (int j = 0; j < 2; ++j) {
      act[j] = j * 64 + lane < Tn;
      mt[j] = s_exp[j * 64 + lane];
      eid[j] = act[j] ? (mt[j].x & 0x3ffffu) : 0u;
    }
    STA(4);
    if (seg == 0) {
#pragma unroll
      for (int j = 0; j < 2; ++j)
        if (act[j]) atomicAdd(&s_deg[mt[j].y], 1u);
      if (lane == 0) atomicMax(&s_misc[0], (u32)T);
      if (ovn) atomicMax(&s_misc[1], ovn);
    }
    *(u32x4*)(smem + O_WRF + tid * 16) = wr0;
    if (tid < 2) *(u32x4*)(smem + O_EA + PCAP * 32 + tid * 16) = u32x4{0u, 0u, 0u, 0u};
#pragma unroll
    for (int j = 0; j < 2; ++j) shv[j] = *(const u32x2*)(sh + (size_t)eid[j] * 8);
    if (seg == 0) {
      STA(5);
      __syncthreads();
      const u32 d = umin(s_deg[lane], 127u);
      u32 gl = 0u, gh = 0u, el = 0xffffffffu, eh = 0xffu;
#pragma unroll
      for (int bit = 6; bit >= 0; --bit) {
        const bool on = (d >> bit) & 1u;
        const u64 B = __builtin_amdgcn_ballot_w64(on);
        const u32 bl = (u32)B, bh = (u32)(B >> 32);
        gl |= on ? 0u : (el & bl);
        gh |= on ? 0u : (eh & bh);
        el &= on ? bl : ~bl;
        eh &= on ? bh : ~bh;
      }
      const u32 rk = __builtin_popcount(gl) + __builtin_popcount(gh) +
                     __builtin_amdgcn_mbcnt_hi(eh, __builtin_amdgcn_mbcnt_lo(el, 0u));
      s_rank[lane] = rk;
      nseg0 = (int)((__builtin_amdgcn_readfirstlane(s_misc[0]) + SEGW - 1) / SEGW);
      nseg0 = nseg0 < 1 ? 1 : nseg0;
      cps = (int)((__builtin_amdgcn_readfirstlane(s_misc[1]) + SEGW - 1) / SEGW);
      nseg = nseg0 + 32 * cps;
      if (wv == 0 && (u32)lane < cntN) {
        const u32 slot = rk >> 3, r8 = rk & 7u;
        s_inv[((slot & 1u) ? 7u - r8 : r8) * 8u + slot] = (u32)lane;
      }
      wavesync();
    }
    STA(6);
    u32 key[2], rk[2], slot[2], comp[2];
#pragma unroll
    for (int j = 0; j < 2; ++j) {
      key[j] = 0u; rk[j] = 0u; slot[j] = 0u; comp[j] = 0u;
      if (j * 64 < Tn) {
        const u32 nr = s_rank[act[j] ? mt[j].y : 0u];
        slot[j] = nr >> 3;
        const u32 r8 = nr & 7u;
        const u32 bk = umin((u32)((float)(mt[j].x >> 18) * bscale), 31u);
        key[j] = ((slot[j] & 1u) ? 7u - r8 : r8) * 32u + bk;
        comp[j] = mt[j].x;
        if (act[j]) rk[j] = atomicAdd(&s_cnt[key[j]], 1u);
      }
    }
    e0[0] = *(const u32x4*)(eattr + (size_t)eid[0] * 32);
    e1[0] = *(const u32x4*)(eattr + (size_t)eid[0] * 32 + 16);
    __syncthreads();
    STA(7);
    u32 ls = 0, lc = 0;
    {
      const u32x4 c = *(const u32x4*)(s_cnt + 4 * lane);
      const u32 p1 = c.x, p2 = p1 + c.y, p3 = p2 + c.z, tot = p3 + c.w;
      const u32 inc = scan64(tot);
      u32 run = 0, prev = 0, adj = 0;
#pragma unroll
      for (int k = 0; k < 8; ++k) {
        const u32 ek = __builtin_amdgcn_readlane(inc, 8 * k + 7);
        const u32 ck = ek - prev;
        ls = (wv == k) ? run : ls;
        lc = (wv == k) ? ck : lc;
        adj = ((lane >> 3) == k) ? run - prev : adj;
        run += ck ? ((ck + 3u) & ~3u) : 4u;
        prev = ek;
      }
      const u32 ex = inc - tot + adj;
      *(u32x4*)(s_base + 4 * lane) = u32x4{ex, ex + p1, ex + p2, ex + p3};
      wavesync();
    }
    u32 kb[2], kn[2];
#pragma unroll
    for (int j = 0; j < 2; ++j) {
      kb[j] = 0u;
      kn[j] = 0u;
      if (j * 64 < Tn) {
        kb[j] = s_base[key[j]];
        kn[j] = act[j] ? s_cnt[key[j]] : 0u;
        if (act[j]) s_tmp[kb[j] + rk[j]] = comp[j];
      }
    }
    e0[1] = *(const u32x4*)(eattr + (size_t)eid[1] * 32);
    e1[1] = *(const u32x4*)(eattr + (size_t)eid[1] * 32 + 16);
    __syncthreads();
    STA(8);
    {
      u32 p[2] = {kb[0], kb[1]};
      const u32 nm = kn[0] > kn[1] ? kn[0] : kn[1];
#pragma clang loop unroll(disable) vectorize(disable) interleave(disable)
      for (u32 k = 0; k < nm; k += 4) {
        u32 t[2][4];
#pragma unroll
        for (int j = 0; j < 2; ++j)
#pragma unroll
          for (int q = 0; q < 4; ++q) t[j][q] = s_tmp[kb[j] + k + q];
#pragma unroll
        for (int j = 0; j < 2; ++j)
#pragma unroll
          for (int q = 0; q < 4; ++q) p[j] += (k + q < kn[j] && t[j][q] < comp[j]) ? 1u : 0u;
      }
#pragma unroll
      for (int j = 0; j < 2; ++j)
        if (act[j]) s_meta[p[j]] = (comp[j] & 0xfffc0000u) | ((u32)(wv * SEGW + j * 64 + lane) << 4) | slot[j];
    }
    const u32 lp = lc ? ((lc + 3u) & ~3u) : 4u;
    if (lane < 4 && lc + lane < lp) {
      const u32 p = ls + lc + lane;
      const u32 sl = lc ? (s_tmp[ls + lc - 1] & 0xfffc0000u) : 0u;
      s_meta[p] = sl | ((u32)PCAP << 4) | 15u;
    }
    __syncthreads();
    STA(9);

    u32x4 wf[8][2];
    {
      const int ng = (int)(lp >> 2);
      const int h = lane >> 5, j = lane & 31;
      Ctx c;
      c.xb = x;
      c.xoff = j * 16;
      c.js = j >> 2;
      c.atl = O_AT + (j & 3) * 8;
      c.mad = O_META + (ls + 2 * h) * 4;
      const bool match = (q4 >> 1) == ((c16 >> 2) & 1);
      c.ead = match ? O_META + (ls + 4 * ((c16 >> 1) & 1) + 2 * (c16 >> 3) + (c16 & 1)) * 4 : O_MISC + 8;
      c.east = match ? 32 : 0;
      c.eao = O_EA + 16 * (q4 & 1);
      c.wra = O_WRF + lane * 16;
      u32x2 m0 = *(const u32x2*)(smem + c.mad);
      u32x2 m1 = *(const u32x2*)(smem + c.mad + 16);
      u32x4 XA[8], XB[8];
      load_x(c, m0, XA);
      const u32 me0 = *(const u32*)(smem + c.ead);
      __builtin_amdgcn_sched_barrier(0);
      u32x4 at[2][2];
      u64 cga = (u64)cg2;
      asm volatile("" : "+s"(cga));
      const __attribute__((address_space(4))) u32* cgp = (const __attribute__((address_space(4))) u32*)cga;
#pragma unroll
      for (int j = 0; j < 2; ++j) {
        at[j][0] = u32x4{0u, 0u, 0u, 0u};
        at[j][1] = at[j][0];
        if (j * 64 < Tn) {
          const float s0 = bflo(shv[j].x), s1 = bfhi(shv[j].x), s2 = bflo(shv[j].y), s3 = bfhi(shv[j].y);
#pragma unroll
          for (int w = 0; w < 4; ++w) {
            float a[4];
#pragma unroll
            for (int u = 0; u < 4; ++u) {
              const u32 c0 = cgp[u * 8 + 0 + (w >> 1)], c1 = cgp[u * 8 + 2 + (w >> 1)];
              const u32 c2 = cgp[u * 8 + 4 + (w >> 1)], c3 = cgp[u * 8 + 6 + (w >> 1)];
              a[u] = ((w & 1) ? bfhi(c0) : bflo(c0)) * s0 + ((w & 1) ? bfhi(c1) : bflo(c1)) * s1 +
                     ((w & 1) ? bfhi(c2) : bflo(c2)) * s2 + ((w & 1) ? bfhi(c3) : bflo(c3)) * s3;
            }
            at[j][w >> 1][2 * (w & 1)] = cvtpk(a[0], a[1]);
            at[j][w >> 1][2 * (w & 1) + 1] = cvtpk(a[2], a[3]);
          }
        }
      }
#pragma unroll
      for (int j = 0; j < 2; ++j) {
        if (j * 64 < Tn) {
          const int p = wv * SEGW + j * 64 + lane;
          u32x4* pa = (u32x4*)(smem + O_AT + p * 32);
          u32x4* pe = (u32x4*)(smem + O_EA + p * 32);
          pa[0] = at[j][0];
          pa[1] = at[j][1];
          pe[0] = e0[j];
          pe[1] = e1[j];
        }
      }
      __syncthreads();
      f32x4 rw[8];
      u32x4 eaf = *(const u32x4*)(smem + c.eao + (me0 & 0x7ff0u) * 2u);
#pragma unroll
      for (int r = 2; r < 8; ++r) rw[r] = f32x4{0.f, 0.f, 0.f, 0.f};
      rw[0] = rwm(smem, c, eaf, 0);
      rw[1] = rwm(smem, c, eaf, 1);
      int g = 0;
      for (; g + 2 < ng; g += 2) {
        step<0>(smem, c, g, m0, m1, eaf, rw, XA, XB, acc);
        step<1>(smem, c, g + 1, m0, m1, eaf, rw, XB, XA, acc);
      }
      if (g + 1 < ng) {
        step<0>(smem, c, g, m0, m1, eaf, rw, XA, XB, acc);
        ++g;
#pragma unroll
        for (int k = 0; k < 8; ++k) XA[k] = XB[k];
#pragma unroll
        for (int r = 0; r < 8; ++r) { rw[r][0] = rw[r][2]; rw[r][1] = rw[r][3]; }
      } else {
#pragma unroll
        for (int r = 2; r < 8; ++r) rw[r] = rwm(smem, c, eaf, r);
      }
      step<2>(smem, c, g, m0, m1, eaf, rw, XA, XB, acc);
      __builtin_amdgcn_sched_barrier(0);
      {
        const char* wp = ws + W_WO + (size_t)(wv * 16 * 64 + lane) * 16;
#pragma unroll
        for (int ks = 0; ks < 8; ++ks) {
          wf[ks][0] = *(const u32x4*)(wp + (ks * 2 + 0) * 1024);
          wf[ks][1] = *(const u32x4*)(wp + (ks * 2 + 1) * 1024);
        }
        __builtin_amdgcn_sched_barrier(0);
      }
    }
    STA(10);
    __syncthreads();
    if (seg + 1 < nseg) continue;

    {
      const u32 loc = s_inv[wv * 8 + ((lane & 31) >> 2)];
      if (loc != 0xffffffffu) {
        char* rowp = smem + O_TILE + (loc * 4 + (lane & 3)) * TROW + (lane >> 5) * 64;
#pragma unroll
        for (int a = 0; a < 16; ++a)
          *(u32x4*)(rowp + (a >> 2) * 128 + (a & 3) * 16) =
              u32x4{cvtpk(acc[0][a], acc[1][a]), cvtpk(acc[2][a], acc[3][a]), cvtpk(acc[4][a], acc[5][a]),
                    cvtpk(acc[6][a], acc[7][a])};
      }
    }
    STA(11);
    __syncthreads();
    STA(12);
    {
      const int cb = 128 * (q4 & 1) + 64 * (q4 >> 1);
      const char* tp = smem + O_TILE + c16 * TROW + cb * 2;
      char* op = out + ((size_t)n0 * 4 + c16) * 512 + (wv * 32 + 8 * q4) * 2;
      const u32 nrows = cntN * 4;
#pragma unroll 2
      for (int rb = 0; rb < 10; ++rb) {
        f32x4 a0 = {0.f, 0.f, 0.f, 0.f}, a1 = {0.f, 0.f, 0.f, 0.f};
#pragma unroll
        for (int ks = 0; ks < 8; ++ks) {
          const u32x4 bf = *(const u32x4*)(tp + rb * 16 * TROW + ks * 16);
          a0 = MFMA16(__builtin_bit_cast(bf16x8, wf[ks][0]), __builtin_bit_cast(bf16x8, bf), a0);
          a1 = MFMA16(__builtin_bit_cast(bf16x8, wf[ks][1]), __builtin_bit_cast(bf16x8, bf), a1);
        }
        if ((u32)(rb * 16 + c16) < nrows) {
          const u32x4 o = {cvtpk(a0[0], a1[0]), cvtpk(a0[1], a1[1]), cvtpk(a0[2], a1[2]), cvtpk(a0[3], a1[3])};
          *(u32x4*)(op + (size_t)rb * 16 * 512) = o;
        }
      }
    }
    STA(13);
    STDUMP();
    return;
  }
}
}

extern "C" void kernel_launch(void* const* d_in, const int* in_sizes, int n_in, void* d_out, int out_size,
                              void* d_ws, size_t ws_size, hipStream_t stream) {
  const int N = in_sizes[0] / 1024;
  const int E = in_sizes[1];
  if (N <= 0) return;
  if (N <= 10240 && E >= 1 && E <= fast::MAXE && ws_size >= fast::W_END) {
    int S = (E + 255) / 256;
    if (S < 1) S = 1;
    fast::bin_kernel<<<256, fast::ANT, 0, stream>>>((const int*)d_in[1], (const int*)d_in[2],
                                                    (const unsigned short*)d_in[6], (const unsigned short*)d_in[7],
                                                    (char*)d_ws, N, E, S, 1.0f / (float)N);
    fast::agg_kernel<<<256, fast::BNT, 0, stream>>>((const char*)d_in[0], (const char*)d_in[3], (const char*)d_in[4],
                                                    (const u32*)d_in[5], (char*)d_out, (char*)d_ws, N, S,
                                                    32.0f / (float)N);
    return;
  }
  int G = (N + gen::MAXN - 1) / gen::MAXN;
  if (G < 256) G = 256;
  if (G > N) G = N;
  size_t gmax = ws_size / ((size_t)gen::NW * 16384);
  if (gmax > 65536) gmax = 65536;
  if (gmax < 1) return;
  for (int b0 = 0; b0 < G; b0 += (int)gmax) {
    const int g = (G - b0 < (int)gmax) ? G - b0 : (int)gmax;
    gen::mace_kernel<<<g, gen::NT, 0, stream>>>(
        (const char*)d_in[0], (const int*)d_in[1], (const int*)d_in[2], (const char*)d_in[3], (const char*)d_in[4],
        (const unsigned short*)d_in[5], (const char*)d_in[6], (const char*)d_in[7], (char*)d_out, (float*)d_ws, N, E,
        b0, G, (float)gen::NB / (float)N);
  }
}
```

```cpp
#include <hip/hip_runtime.h>
#include <hip/hip_bf16.h>
#include <stdint.h>

typedef unsigned int u32;
typedef unsigned long long u64;
typedef __attribute__((ext_vector_type(8))) short bf16x8;
typedef __attribute__((ext_vector_type(2))) __bf16 bf16x2_t;
typedef __attribute__((ext_vector_type(2))) float f32x2;
typedef __attribute__((ext_vector_type(4))) float f32x4;
typedef __attribute__((ext_vector_type(16))) float f32x16;
typedef __attribute__((ext_vector_type(2))) unsigned int u32x2;
typedef __attribute__((ext_vector_type(4))) unsigned int u32x4;

#define DI __device__ __forceinline__
#define ST(k) do { } while (0)
#define MFMA32(a, b, c) __builtin_amdgcn_mfma_f32_32x32x16_bf16((a), (b), (c), 0, 0, 0)
#define MFMA16(a, b, c) __builtin_amdgcn_mfma_f32_16x16x32_bf16((a), (b), (c), 0, 0, 0)

DI u32 cvtpk(float a, float b) {
  f32x2 v = {a, b};
  bf16x2_t r = __builtin_convertvector(v, bf16x2_t);
  return __builtin_bit_cast(u32, r);
}
DI float bflo(u32 v) { return __builtin_bit_cast(float, v << 16); }
DI float bfhi(u32 v) { return __builtin_bit_cast(float, v & 0xffff0000u); }
DI u32 mbcnt(u64 m) { return __builtin_amdgcn_mbcnt_hi((u32)(m >> 32), __builtin_amdgcn_mbcnt_lo((u32)m, 0u)); }
DI u32 umin(u32 a, u32 b) { return a < b ? a : b; }

namespace gen {
constexpr int NT = 768, NW = 12, NRG = 6, NB = 32;
constexpr int MAXN = 40;
constexpr int CAP = 944;
constexpr int LCAP = CAP + 48;
constexpr int CAPW = 128;
constexpr int WMIN = 512;
constexpr int SU = 9;
constexpr int TROW = 528;
constexpr int PFROWS = 1536;

constexpr int O_CNT = 0;
constexpr int O_BASE = O_CNT + 192 * 4;
constexpr int O_DEG = O_BASE + 192 * 4;
constexpr int O_MAP = O_DEG + 64 * 4;
constexpr int O_INV = O_MAP + 64 * 4;
constexpr int O_CGT = O_INV + 64 * 4;
constexpr int O_ZERO = O_CGT + 64 * 4;
constexpr int O_MISC = O_ZERO + 64;
constexpr int O_WR = O_MISC + 64;
constexpr int O_META = O_WR + 8192;
constexpr int O_AT = O_META + LCAP * 4;
constexpr int O_EA = O_AT + LCAP * 32;
constexpr int O_TILE = O_EA + LCAP * 32;
constexpr int O_CANDQ = O_TILE;
constexpr int O_CANDI = O_CANDQ + NW * CAPW * 16;
constexpr int O_HITE = O_CANDI + NW * CAPW * 4;
constexpr int O_HITL = O_HITE + 1024 * 4;
constexpr int O_TMP = O_HITL + 1024 * 4;
constexpr int LDS_BYTES = O_TILE + MAXN * 4 * TROW;
static_assert(O_TMP + LCAP * 8 <= LDS_BYTES, "alias region too small");
static_assert(LDS_BYTES <= 163840, "LDS");
static_assert((O_AT % 16) == 0 && (O_EA % 16) == 0 && (O_TILE % 16) == 0 && (O_WR % 16) == 0, "align");

struct MainCtx {
  const char* xb;
  u32 xoff;
  u32 myslot;
  u32 atlane;
  u32 metaaddr;
  u32 eaaddr, eastride;
  u32 wraddr;
};

DI void load_block(const char* xb, u32 xoff, u32 m0, u32 m1, u32x2 (&buf)[8]) {
  const u32 o0 = (m0 & 0xfffff800u) | xoff, o1 = (m1 & 0xfffff800u) | xoff;
  const char* p0 = xb + (size_t)o0;
  const char* p1 = xb + (size_t)o1;
#pragma unroll
  for (int u = 0; u < 4; ++u) buf[u] = *(const u32x2*)(p0 + u * 512);
#pragma unroll
  for (int u = 0; u < 4; ++u) buf[4 + u] = *(const u32x2*)(p1 + u * 512);
}

DI u32x4 make_z(const u32x2 (&buf)[8], int r, float s0, float s1) {
  u32x4 z;
#pragma unroll
  for (int i = 0; i < 2; ++i) {
    const float s = i ? s1 : s0;
#pragma unroll
    for (int up = 0; up < 2; ++up) {
      const u32 a = (r & 2) ? buf[4 * i + 2 * up].y : buf[4 * i + 2 * up].x;
      const u32 b = (r & 2) ? buf[4 * i + 2 * up + 1].y : buf[4 * i + 2 * up + 1].x;
      z[2 * i + up] = (r & 1) ? cvtpk(bfhi(a) * s, bfhi(b) * s) : cvtpk(bflo(a) * s, bflo(b) * s);
    }
  }
  return z;
}

template <bool PF>
DI void step(char* smem, const MainCtx& c, int g, u32x4& mc, u32x4& eaf, u32x2 (&xb0)[8], u32x2 (&xb1)[8],
             f32x16 (&acc)[4]) {
  const u32 ab = c.atlane + g * 256;
  u32x2 bt[4];
#pragma unroll
  for (int k = 0; k < 4; ++k) {
    const u32 a = ((mc[k] & 15u) == c.myslot) ? ab + k * 32 : (u32)O_ZERO;
    bt[k] = *(const u32x2*)(smem + a);
  }
  if (PF) mc = *(const u32x4*)(smem + c.metaaddr + (g + 1) * 32);
  f32x4 rw[4];
  const f32x4 z4 = {0.f, 0.f, 0.f, 0.f};
  u32 wra = c.wraddr;
  asm volatile("" : "+v"(wra));
#pragma unroll
  for (int r = 0; r < 4; ++r) {
    const u32x4 wr = *(const u32x4*)(smem + wra + r * 1024);
    rw[r] = MFMA16(__builtin_bit_cast(bf16x8, eaf), __builtin_bit_cast(bf16x8, wr), z4);
  }
  if (PF) eaf = *(const u32x4*)(smem + c.eaaddr + (g + 1) * c.eastride);
  {
    const u32x4 b0 = {bt[0].x, bt[0].y, bt[1].x, bt[1].y};
#pragma unroll
    for (int r = 0; r < 4; ++r) {
      const u32x4 z = make_z(xb0, r, rw[r][0], rw[r][1]);
      acc[r] = MFMA32(__builtin_bit_cast(bf16x8, z), __builtin_bit_cast(bf16x8, b0), acc[r]);
    }
    if (PF) load_block(c.xb, c.xoff, mc[0], mc[1], xb0);
  }
  {
    const u32x4 b1 = {bt[2].x, bt[2].y, bt[3].x, bt[3].y};
#pragma unroll
    for (int r = 0; r < 4; ++r) {
      const u32x4 z = make_z(xb1, r, rw[r][2], rw[r][3]);
      acc[r] = MFMA32(__builtin_bit_cast(bf16x8, z), __builtin_bit_cast(bf16x8, b1), acc[r]);
    }
    if (PF) load_block(c.xb, c.xoff, mc[2], mc[3], xb1);
  }
}

__global__ __launch_bounds__(NT, 3) void mace_kernel(
    const char* __restrict__ x, const int* __restrict__ src, const int* __restrict__ dst,
    const char* __restrict__ sh, const char* __restrict__ eattr, const unsigned short* __restrict__ cg,
    const char* __restrict__ w_r, const char* __restrict__ w_out, char* __restrict__ out,
    float* __restrict__ ws, int N, int E, int b0, int G, float bscale) {
  __shared__ __attribute__((aligned(16))) char smem[LDS_BYTES];
  const int tid0 = threadIdx.x;
  const int wv = __builtin_amdgcn_readfirstlane(tid0 >> 6);
  const int rg = wv >> 1, hf = wv & 1;
  const int b = b0 + blockIdx.x;
  const u32 n0 = (u32)(((u64)b * (u64)N) / (u64)G);
  const u32 cntN = (u32)((((u64)b + 1) * (u64)N) / (u64)G) - n0;

  u32* s_cnt = (u32*)(smem + O_CNT);
  u32* s_base = (u32*)(smem + O_BASE);
  u32* s_deg = (u32*)(smem + O_DEG);
  u32* s_map = (u32*)(smem + O_MAP);
  u32* s_inv = (u32*)(smem + O_INV);
  float* s_cgt = (float*)(smem + O_CGT);
  u32* s_misc = (u32*)(smem + O_MISC);
  u32* s_hitE = (u32*)(smem + O_HITE);
  u32* s_hitL = (u32*)(smem + O_HITL);
  u64* s_tmp = (u64*)(smem + O_TMP);
  u32* s_meta = (u32*)(smem + O_META);

  if (wv < 2) {
    const int lane = tid0 & 63;
    const int q4 = lane >> 4, c16 = lane & 15;
    u32x4 wr[4];
    u32x2 L[8];
    const char* p = w_r + (size_t)(8 * (q4 & 1)) * 512 + (hf * 128 + 64 * (q4 >> 1) + 4 * c16) * 2;
#pragma unroll
    for (int j = 0; j < 8; ++j) L[j] = *(const u32x2*)(p + j * 512);
#pragma unroll
    for (int jj = 0; jj < 4; ++jj) {
      wr[0][jj] = __builtin_amdgcn_perm(L[2 * jj + 1].x, L[2 * jj].x, 0x05040100u);
      wr[1][jj] = __builtin_amdgcn_perm(L[2 * jj + 1].x, L[2 * jj].x, 0x07060302u);
      wr[2][jj] = __builtin_amdgcn_perm(L[2 * jj + 1].y, L[2 * jj].y, 0x05040100u);
      wr[3][jj] = __builtin_amdgcn_perm(L[2 * jj + 1].y, L[2 * jj].y, 0x07060302u);
    }
#pragma unroll
    for (int r = 0; r < 4; ++r) *(u32x4*)(smem + O_WR + hf * 4096 + r * 1024 + lane * 16) = wr[r];
  }
  if (wv == 2) {
    const int tid = tid0 & 63;
    const int w = tid >> 4, u = (tid >> 2) & 3, v = tid & 3;
    s_cgt[tid] = bflo((u32)cg[u * 16 + v * 4 + w]);
    s_map[tid] = (u32)(tid % NRG) | ((u32)(tid / NRG) << 8);
    const u32 l = (u32)((tid & 7) * NRG + (tid >> 3));
    s_inv[tid] = (tid < NRG * 8 && l < cntN) ? l : 0xffffffffu;
    if (tid < 16) ((u32*)(smem + O_ZERO))[tid] = 0u;
  }

  bool first = true, again = false;
  int pos = 0;
  int W = (E + 3) & ~3;
  for (;;) {
    if (again) __syncthreads();
    again = true;
    int tid = tid0;
    asm volatile("" : "+v"(tid));
    const int lane = tid & 63;
    const int q4 = lane >> 4, c16 = lane & 15, h = lane >> 5;
    u32 pfv = 0;
    {
      const u32 line = (u32)(b >> 3) * NT + tid;
      if (first && line < (u32)PFROWS * 16u && line < (u32)N * 16u) pfv = *(const u32*)(x + (size_t)line * 128);
    }
    if (tid < 192) s_cnt[tid] = 0u;
    if (tid >= 192 && tid < 256) s_deg[tid - 192] = 0u;
    if (tid >= 256 && tid < 258) s_misc[tid - 256] = 0u;
    __syncthreads();
    const int end = (E - pos < W) ? E : pos + W;

    {
      const u32x4* dq = (const u32x4*)dst;
      u32x4* cq = (u32x4*)(smem + O_CANDQ) + wv * CAPW;
      u32* ci = (u32*)(smem + O_CANDI) + wv * CAPW;
      const int q0 = pos >> 2, qf = end >> 2;
      u32 nc = 0;
      for (int qb = q0 + wv * 64; qb < qf; qb += NT * SU) {
        u32x4 v[SU];
#pragma unroll
        for (int k = 0; k < SU; ++k) {
          const int q = qb + k * NT + lane;
          v[k] = dq[q < qf ? q : qf - 1];
        }
#pragma unroll
        for (int k = 0; k < SU; ++k) {
          const int q = qb + k * NT + lane;
          const u32 m = umin(umin(v[k].x - n0, v[k].y - n0), umin(v[k].z - n0, v[k].w - n0));
          const bool hit = (m < cntN) && (q < qf);
          const u64 mask = __ballot(hit);
          if (mask) {
            const u32 p = nc + mbcnt(mask);
            if (hit && p < (u32)CAPW) { cq[p] = v[k]; ci[p] = (u32)q; }
            nc += (u32)__popcll(mask);
          }
        }
      }
      if (wv == NW - 1 && (end & 3)) {
        u32x4 v;
        const int e4 = qf * 4;
        v.x = (u32)dst[e4];
        v.y = (e4 + 1 < end) ? (u32)dst[e4 + 1] : n0 - 1u;
        v.z = (e4 + 2 < end) ? (u32)dst[e4 + 2] : n0 - 1u;
        v.w = n0 - 1u;
        if (lane == 0 && nc < (u32)CAPW) { cq[nc] = v; ci[nc] = (u32)qf; }
        nc += 1;
      }
      if (nc > (u32)CAPW) { if (lane == 0) s_misc[1] = 1u; nc = CAPW; }
      __builtin_amdgcn_wave_barrier();
      for (u32 i0 = 0; i0 < nc; i0 += 64) {
        const u32 i = i0 + lane;
        const bool valid = i < nc;
        const u32x4 v = cq[valid ? i : 0];
        const u32 q = ci[valid ? i : 0];
        const u32 t[4] = {v.x - n0, v.y - n0, v.z - n0, v.w - n0};
        u64 mk[4];
        u32 tot = 0;
#pragma unroll
        for (int k = 0; k < 4; ++k) { mk[k] = __ballot(valid && t[k] < cntN); tot += (u32)__popcll(mk[k]); }
        u32 bs = 0;
        if (lane == 0) bs = atomicAdd(&s_misc[0], tot);
        bs = __builtin_amdgcn_readfirstlane(bs);
#pragma unroll
        for (int k = 0; k < 4; ++k) {
          const u32 p = bs + mbcnt(mk[k]);
          if (valid && t[k] < cntN && p < (u32)CAP) {
            s_hitE[p] = 4 * q + k;
            s_hitL[p] = t[k];
            atomicAdd(&s_deg[t[k]], 1u);
          }
          bs += (u32)__popcll(mk[k]);
        }
      }
    }
    asm volatile("" ::"v"(pfv));
    __syncthreads();
    const u32 nh = __builtin_amdgcn_readfirstlane(s_misc[0]);
    if (nh > (u32)CAP || __builtin_amdgcn_readfirstlane(s_misc[1]) != 0u) {
      W = ((W >> 1) + 3) & ~3;
      if (W < WMIN) W = WMIN;
      continue;
    }

    u32 he[2], hl[2], hs[2];
    u32x2 hsh[2];
    u32x4 hea[2][2];
    bool act[2];
#pragma unroll
    for (int j = 0; j < 2; ++j) {
      const u32 i = tid + j * NT;
      act[j] = i < nh;
      he[j] = 0u; hl[j] = 0u; hs[j] = 0u;
      hsh[j] = u32x2{0u, 0u};
      hea[j][0] = u32x4{0u, 0u, 0u, 0u};
      hea[j][1] = u32x4{0u, 0u, 0u, 0u};
      if (j == 0 ? nh > 0u : nh > (u32)NT) {
        const u32 ii = act[j] ? i : 0;
        he[j] = s_hitE[ii];
        hl[j] = s_hitL[ii];
        hs[j] = (u32)src[he[j]];
        hsh[j] = *(const u32x2*)(sh + (size_t)he[j] * 8);
        hea[j][0] = *(const u32x4*)(eattr + (size_t)he[j] * 32);
        hea[j][1] = *(const u32x4*)(eattr + (size_t)he[j] * 32 + 16);
      }
    }
    if (first && wv == NW - 1) {
      const u32 d = s_deg[lane];
      u32 rank = 0;
      for (u32 j = 0; j < cntN; ++j) {
        const u32 dj = __builtin_amdgcn_readlane(d, j);
        rank += (dj > d || (dj == d && j < (u32)lane)) ? 1u : 0u;
      }
      const u32 slot = rank / NRG, r = rank % NRG;
      const u32 g = (slot & 1) ? (NRG - 1 - r) : r;
      s_inv[lane] = 0xffffffffu;
      __builtin_amdgcn_wave_barrier();
      if ((u32)lane < cntN) { s_map[lane] = g | (slot << 8); s_inv[g * 8 + slot] = lane; }
    }
    __syncthreads();

    u32 key[2], rk[2], mp[2];
#pragma unroll
    for (int j = 0; j < 2; ++j) {
      key[j] = 0u; rk[j] = 0u; mp[j] = 0u;
      if (j == 0 ? nh > 0u : nh > (u32)NT) {
        mp[j] = s_map[hl[j]];
        u32 bk = (u32)((float)hs[j] * bscale);
        bk = umin(bk, NB - 1);
        key[j] = (mp[j] & 255u) * NB + bk;
        if (act[j]) rk[j] = atomicAdd(&s_cnt[key[j]], 1u);
      }
    }
    __syncthreads();
    u32 ls = 0, lc = 0;
    {
      u32 v[3], inc[3];
#pragma unroll
      for (int k = 0; k < 3; ++k) {
        v[k] = s_cnt[k * 64 + lane];
        u32 t = v[k];
        t += (u32)__builtin_amdgcn_update_dpp(0, (int)t, 0x111, 0xf, 0xf, true);
        t += (u32)__builtin_amdgcn_update_dpp(0, (int)t, 0x112, 0xf, 0xf, true);
        t += (u32)__builtin_amdgcn_update_dpp(0, (int)t, 0x114, 0xf, 0xf, true);
        t += (u32)__builtin_amdgcn_update_dpp(0, (int)t, 0x118, 0xf, 0xf, true);
        t += (u32)__builtin_amdgcn_update_dpp(0, (int)t, 0x142, 0xa, 0xf, false);
        inc[k] = t;
      }
      u32 run = 0;
#pragma unroll
      for (int k = 0; k < 3; ++k) {
        const u32 c0 = __builtin_amdgcn_readlane(inc[k], 31);
        const u32 c1 = __builtin_amdgcn_readlane(inc[k], 63);
        const u32 s0 = run, s1 = run + ((c0 + 7u) & ~7u);
        run = s1 + ((c1 + 7u) & ~7u);
        ls = (rg == 2 * k) ? s0 : ls;
        lc = (rg == 2 * k) ? c0 : lc;
        ls = (rg == 2 * k + 1) ? s1 : ls;
        lc = (rg == 2 * k + 1) ? c1 : lc;
        s_base[k * 64 + lane] = (h ? s1 : s0) + inc[k] - v[k];
      }
      __builtin_amdgcn_wave_barrier();
    }
    u32 kb[2];
    u64 comp[2];
#pragma unroll
    for (int j = 0; j < 2; ++j) {
      kb[j] = 0u; comp[j] = 0ull;
      if (j == 0 ? nh > 0u : nh > (u32)NT) {
        kb[j] = s_base[key[j]];
        comp[j] = ((u64)hs[j] << 32) | he[j];
        if (act[j]) s_tmp[kb[j] + rk[j]] = comp[j];
      }
    }
    __syncthreads();
#pragma unroll
    for (int j = 0; j < 2; ++j) {
      if ((j == 0 || nh > (u32)NT) && act[j]) {
        const u32 n = s_cnt[key[j]];
        u32 p = kb[j];
        for (u32 k = 0; k < n; ++k) p += (s_tmp[kb[j] + k] < comp[j]) ? 1u : 0u;
        s_meta[p] = (hs[j] << 11) | (mp[j] >> 8);
        const float s0 = bflo(hsh[j].x), s1 = bfhi(hsh[j].x), s2 = bflo(hsh[j].y), s3 = bfhi(hsh[j].y);
        u32 at[8];
#pragma unroll
        for (int w = 0; w < 4; ++w) {
          float a[4];
#pragma unroll
          for (int u = 0; u < 4; ++u) {
            const f32x4 cv = *(const f32x4*)(s_cgt + w * 16 + u * 4);
            a[u] = cv.x * s0 + cv.y * s1 + cv.z * s2 + cv.w * s3;
          }
          at[2 * w] = cvtpk(a[0], a[1]);
          at[2 * w + 1] = cvtpk(a[2], a[3]);
        }
        u32x4* pa = (u32x4*)(smem + O_AT + p * 32);
        pa[0] = u32x4{at[0], at[1], at[2], at[3]};
        pa[1] = u32x4{at[4], at[5], at[6], at[7]};
        u32x4* pe = (u32x4*)(smem + O_EA + p * 32);
        pe[0] = hea[j][0];
        pe[1] = hea[j][1];
      }
    }
    if (hf == 0 && lc > 0) {
      const u32 p = ls + lc + lane;
      if (p < ls + ((lc + 7u) & ~7u)) {
        const u32 sl = (u32)(s_tmp[ls + lc - 1] >> 32);
        s_meta[p] = (sl << 11) | 15u;
        const u32x4 zz = {0u, 0u, 0u, 0u};
        u32x4* pe = (u32x4*)(smem + O_EA + p * 32);
        pe[0] = zz;
        pe[1] = zz;
      }
    }
    __syncthreads();

    f32x16 acc[4];
    if (first) {
#pragma unroll
      for (int r = 0; r < 4; ++r)
#pragma unroll
        for (int i = 0; i < 16; ++i) acc[r][i] = 0.f;
    } else {
      const f32x4* wsp = (const f32x4*)ws + ((size_t)blockIdx.x * NW + wv) * 1024 + lane;
#pragma unroll
      for (int r = 0; r < 4; ++r)
#pragma unroll
        for (int i = 0; i < 4; ++i) {
          const f32x4 t = wsp[(r * 4 + i) * 64];
          acc[r][4 * i] = t.x; acc[r][4 * i + 1] = t.y; acc[r][4 * i + 2] = t.z; acc[r][4 * i + 3] = t.w;
        }
    }
    const int ng = (int)((lc + 7u) >> 3);
    if (ng > 0) {
      MainCtx c;
      c.xb = x;
      c.xoff = hf * 256 + (lane & 31) * 8;
      c.myslot = (lane & 31) >> 2;
      c.atlane = O_AT + (lane & 3) * 8 + (ls + 4 * h) * 32;
      c.metaaddr = O_META + (ls + 4 * h) * 4;
      const bool match = (q4 >> 1) == ((c16 >> 2) & 1);
      c.eaaddr = match ? O_EA + (ls + 4 * (c16 >> 3) + (c16 & 3)) * 32 + 16 * (q4 & 1) : O_ZERO;
      c.eastride = match ? 256 : 0;
      c.wraddr = O_WR + hf * 4096 + lane * 16;
      u32x4 mc = *(const u32x4*)(smem + c.metaaddr);
      u32x4 eaf = *(const u32x4*)(smem + c.eaaddr);
      u32x2 xb0[8], xb1[8];
      load_block(c.xb, c.xoff, mc[0], mc[1], xb0);
      load_block(c.xb, c.xoff, mc[2], mc[3], xb1);
      int g = 0;
      for (; g < ng - 1; ++g) step<true>(smem, c, g, mc, eaf, xb0, xb1, acc);
      step<false>(smem, c, g, mc, eaf, xb0, xb1, acc);
    }
    if (end >= E) {

  u32x4 wf[8][2];
#pragma unroll
  for (int ks = 0; ks < 8; ++ks) { wf[ks][0] = u32x4{0u, 0u, 0u, 0u}; wf[ks][1] = u32x4{0u, 0u, 0u, 0u}; }
  if (wv < 8) {
    const int cb = 128 * (q4 & 1) + 64 * (q4 >> 1);
    const char* p = w_out + (size_t)cb * 512 + (wv * 32 + 2 * c16) * 2;
#pragma unroll
    for (int ks = 0; ks < 8; ++ks) {
      u32 L[8];
#pragma unroll
      for (int j = 0; j < 8; ++j) L[j] = *(const u32*)(p + (ks * 8 + j) * 512);
#pragma unroll
      for (int jj = 0; jj < 4; ++jj) {
        wf[ks][0][jj] = __builtin_amdgcn_perm(L[2 * jj + 1], L[2 * jj], 0x05040100u);
        wf[ks][1][jj] = __builtin_amdgcn_perm(L[2 * jj + 1], L[2 * jj], 0x07060302u);
      }
    }
  }
  {
    const u32 loc = s_inv[rg * 8 + ((lane & 31) >> 2)];
    if (loc != 0xffffffffu) {
      char* rowp = smem + O_TILE + (loc * 4 + (lane & 3)) * TROW + (hf * 128 + 16 * h) * 2;
#pragma unroll
      for (int qq = 0; qq < 4; ++qq) {
        u32 d[8];
#pragma unroll
        for (int a = 0; a < 4; ++a) {
          d[2 * a] = cvtpk(acc[0][4 * qq + a], acc[1][4 * qq + a]);
          d[2 * a + 1] = cvtpk(acc[2][4 * qq + a], acc[3][4 * qq + a]);
        }
        u32x4* pp = (u32x4*)(rowp + qq * 64);
        pp[0] = u32x4{d[0], d[1], d[2], d[3]};
        pp[1] = u32x4{d[4], d[5], d[6], d[7]};
      }
    }
  }
  __syncthreads();
  if (wv < 8) {
    const int cb = 128 * (q4 & 1) + 64 * (q4 >> 1);
    const char* tp = smem + O_TILE + c16 * TROW + cb * 2;
    char* op = out + ((size_t)n0 * 4 + c16) * 512 + (wv * 32 + 8 * q4) * 2;
    const u32 nrows = cntN * 4;
#pragma unroll 2
    for (int rb = 0; rb < MAXN * 4 / 16; ++rb) {
      f32x4 a0 = {0.f, 0.f, 0.f, 0.f}, a1 = {0.f, 0.f, 0.f, 0.f};
#pragma unroll
      for (int ks = 0; ks < 8; ++ks) {
        const u32x4 bf = *(const u32x4*)(tp + rb * 16 * TROW + ks * 16);
        a0 = MFMA16(__builtin_bit_cast(bf16x8, wf[ks][0]), __builtin_bit_cast(bf16x8, bf), a0);
        a1 = MFMA16(__builtin_bit_cast(bf16x8, wf[ks][1]), __builtin_bit_cast(bf16x8, bf), a1);
      }
      if ((u32)(rb * 16 + c16) < nrows) {
        const u32x4 o = {cvtpk(a0[0], a1[0]), cvtpk(a0[1], a1[1]), cvtpk(a0[2], a1[2]), cvtpk(a0[3], a1[3])};
        *(u32x4*)(op + (size_t)rb * 16 * 512) = o;
      }
    }
  }
  return;
    }
    int ln = tid0 & 63;
    asm volatile("" : "+v"(ln));
    f32x4* wsp = (f32x4*)ws + ((size_t)blockIdx.x * NW + wv) * 1024 + ln;
#pragma unroll
    for (int r = 0; r < 4; ++r)
#pragma unroll
      for (int i = 0; i < 4; ++i)
        wsp[(r * 4 + i) * 64] = f32x4{acc[r][4 * i], acc[r][4 * i + 1], acc[r][4 * i + 2], acc[r][4 * i + 3]};
    pos = end;
    first = false;
  }
}

}

namespace fast {
#define CT 1
constexpr int ANT = 640;
constexpr int BNT = 512;
constexpr int SEGW = 128;
constexpr int LCAP = 8 * SEGW + 48;
constexpr int TROW = 528;
constexpr int MAXE = 256 * ANT;
constexpr size_t W_SLOT = 0;
constexpr size_t W_OVN = W_SLOT + 256 * 256 * 64;
constexpr size_t W_WR = W_OVN + 1024;
constexpr size_t W_WO = W_WR + 8192;
constexpr size_t W_OVF = W_WO + 131072;
constexpr size_t W_END = W_OVF + (size_t)MAXE * 8;
constexpr u32 INV = 0xffffffffu;
constexpr int O_CNT = 0;
constexpr int O_BASE = O_CNT + 1024;
constexpr int O_DEG = O_BASE + 1024;
constexpr int O_RANK = O_DEG + 256;
constexpr int O_INV = O_RANK + 256;
constexpr int O_ZERO = O_INV + 256;
constexpr int O_MISC = O_ZERO + 64;
constexpr int O_WRF = O_MISC + 64;
constexpr int O_EXP = O_WRF + 8192;
constexpr int O_TMP = O_EXP + 8 * SEGW * 8;
constexpr int O_META = O_TMP + LCAP * 4;
constexpr int O_AT = O_META + LCAP * 4;
constexpr int PCAP = 8 * SEGW;
constexpr int O_EA = O_AT + PCAP * 32;
constexpr int O_TILE = O_AT;
constexpr int LDS_BYTES = O_TILE + 160 * TROW;
static_assert(O_EA + PCAP * 32 + 32 <= LDS_BYTES, "LDS");
static_assert(LDS_BYTES <= 163840, "LDS");
static_assert((O_AT % 16) == 0 && (O_EA % 16) == 0 && (O_TILE % 16) == 0 && (O_META % 16) == 0, "align");

#define DPPADD(t, ctrl, rmask, bc) t += (u32)__builtin_amdgcn_update_dpp(0, (int)(t), ctrl, rmask, 0xf, bc)
DI u32 scan16(u32 t) {
  DPPADD(t, 0x111, 0xf, true);
  DPPADD(t, 0x112, 0xf, true);
  DPPADD(t, 0x114, 0xf, true);
  DPPADD(t, 0x118, 0xf, true);
  return t;
}
DI u32 scan32(u32 t) { t = scan16(t); DPPADD(t, 0x142, 0xa, false); return t; }
DI u32 scan64(u32 t) { t = scan32(t); DPPADD(t, 0x143, 0xc, false); return t; }
DI void wavesync() { __builtin_amdgcn_fence(__ATOMIC_RELEASE, "wavefront"); __builtin_amdgcn_wave_barrier(); __builtin_amdgcn_fence(__ATOMIC_ACQUIRE, "wavefront"); }

__global__ __launch_bounds__(ANT) void bin_kernel(
    const int* __restrict__ src, const int* __restrict__ dst, const unsigned short* __restrict__ w_r,
    const unsigned short* __restrict__ w_out, char* __restrict__ ws, int N, int E, int S, float invN) {
  __shared__ __attribute__((aligned(16))) u32 s_cnt[260];
  __shared__ __attribute__((aligned(16))) u32x4 s_img[1024];
  const int tid0 = threadIdx.x, tid = tid0, lane = tid & 63;
  const int wv = __builtin_amdgcn_readfirstlane(tid >> 6);
  const int i = blockIdx.x;
  ST(0);
  const int e = i * S + tid;
  const bool act = tid < S && e < E;
  const int ec = e < E ? e : E - 1;
  u32 dn = (u32)dst[ec], sn = (u32)src[ec];
  u32 pf0;
  {
    u64 pc, pa;
    asm volatile(
        "s_getpc_b64 s[40:41]\n\t"
        "s_add_u32 s42, s40, _ZN4fast10agg_kernelEPKcS1_S1_PKjPcS4_iif@rel32@lo+4\n\t"
        "s_addc_u32 s43, s41, _ZN4fast10agg_kernelEPKcS1_S1_PKjPcS4_iif@rel32@hi+12\n\t"
        "s_mov_b64 %0, s[40:41]\n\t"
        "s_mov_b64 %1, s[42:43]"
        : "=s"(pc), "=s"(pa)
        :
        : "s40", "s41", "s42", "s43", "scc");
    const char* a = (const char*)pa + umin((u32)tid, 111u) * 128u;
    if (tid >= 112 && tid < 121) a = (const char*)pc + (tid - 112) * 128;
    pf0 = *(const __attribute__((address_space(1))) u32*)a;
  }
  if (tid < 260) s_cnt[tid] = 0u;
  s_img[tid] = u32x4{INV, INV, INV, INV};
  if (tid < 1024 - ANT) s_img[tid + ANT] = u32x4{INV, INV, INV, INV};
  if (wv == 9 && lane < 34) {
    const unsigned short* p;
    char* o;
    if (lane < 32) {
      const int id = i * 32 + lane;
      const int l2 = id & 63, h = (id >> 6) & 1, ks = (id >> 7) & 7, w8 = id >> 10;
      const int q4 = l2 >> 4, c16 = l2 & 15;
      p = w_out + (128 * (q4 & 1) + 64 * (q4 >> 1) + 8 * ks) * 256 + w8 * 32 + 2 * c16 + h;
      o = ws + W_WO + (size_t)id * 16;
    } else {
      const int id = i * 2 + (lane - 32);
      const int l2 = id & 63, r = id >> 6;
      const int kq = l2 >> 4, j = l2 & 15;
      p = w_r + (8 * (kq & 1)) * 256 + 8 * (16 * (kq >> 1) + j) + r;
      o = ws + W_WR + (size_t)id * 16;
    }
    u32 v[8];
#pragma unroll
    for (int k = 0; k < 8; ++k) v[k] = p[k * 256];
    *(u32x4*)o = u32x4{v[0] | (v[1] << 16), v[2] | (v[3] << 16), v[4] | (v[5] << 16), v[6] | (v[7] << 16)};
  }
  __syncthreads();
  asm volatile("" : "+v"(dn), "+v"(sn));
  u32 b = (u32)((float)(dn * 256u + 255u) * invN);
  b = umin(b, 255u);
  if ((((b + 1u) * (u32)N) >> 8) <= dn) b += 1u;
  if (((b * (u32)N) >> 8) > dn) b -= 1u;
  b = umin(b, 255u);
  const u32 loc = dn - ((b * (u32)N) >> 8);
  if (act) {
    const u32 rec = (sn << 16) | ((u32)tid << 6) | loc;
    const u32 rk = atomicAdd(&s_cnt[b], 1u);
    if (rk < 16u) {
      ((u32*)s_img)[b * 16u + rk] = rec;
    } else {
      const u32 o = atomicAdd(&s_cnt[256], 1u);
      ((u32x2*)(ws + W_OVF))[i * ANT + o] = u32x2{rec, b};
    }
  }
  __syncthreads();
  u32x4* po = (u32x4*)(ws + W_SLOT) + i * 4;
  po[(tid >> 2) * 1024 + (tid & 3)] = s_img[tid];
  if (tid < 1024 - ANT) po[((tid + ANT) >> 2) * 1024 + (tid & 3)] = s_img[tid + ANT];
  if (tid == ANT - 1) ((u32*)(ws + W_OVN))[i] = s_cnt[256];
  asm volatile("" ::"v"(pf0));
  ST(1);
}

struct Ctx {
  const char* xb;
  u32 xoff;
  u32 js;
  u32 atl;
  u32 mad;
  u32 ead, east;
  u32 eao;
  u32 wra;
};

DI void load_x(const Ctx& c, u32x2 m, u32x4 (&X)[8]) {
#pragma unroll
  for (int i = 0; i < 2; ++i) {
    const char* p = c.xb + (size_t)(((m[i] >> 18) << 11) | c.xoff);
#pragma unroll
    for (int u = 0; u < 4; ++u) X[4 * i + u] = *(const u32x4*)(p + u * 512);
  }
}

DI f32x4 rwm(char* smem, const Ctx& c, const u32x4& eaf, int r) {
  const u32x4 w = *(const u32x4*)(smem + c.wra + r * 1024);
  const f32x4 z4 = {0.f, 0.f, 0.f, 0.f};
  return MFMA16(__builtin_bit_cast(bf16x8, eaf), __builtin_bit_cast(bf16x8, w), z4);
}

template <int PH>
DI void step(char* smem, const Ctx& c, int g, u32x2& m0, u32x2& m1, u32x4& eaf, f32x4 (&rw)[8],
             const u32x4 (&X)[8], u32x4 (&Y)[8], f32x16 (&acc)[8]) {
  u32x4 bt;
  u32 me = 0;
#pragma unroll
  for (int i = 0; i < 2; ++i) {
    const u32 a = ((m0[i] & 15u) == c.js) ? c.atl + (m0[i] & 0x7ff0u) * 2u : (u32)O_ZERO;
    const u32x2 t = *(const u32x2*)(smem + a);
    bt[2 * i] = t.x;
    bt[2 * i + 1] = t.y;
  }
  if (PH != 2) {
    asm volatile("" ::"v"(X[0]), "v"(X[1]), "v"(X[2]), "v"(X[3]), "v"(X[4]), "v"(X[5]), "v"(X[6]), "v"(X[7]));
    load_x(c, m1, Y);
    m0 = m1;
    m1 = *(const u32x2*)(smem + c.mad + (g + 2) * 16);
    __builtin_amdgcn_sched_barrier(0);
  }
#pragma unroll
  for (int r = 0; r < 8; ++r) {
    if (PH == 0 && r < 6) rw[r + 2] = rwm(smem, c, eaf, r + 2);
    if (PH == 1 && r == 2) me = *(const u32*)(smem + c.ead + ((g + 1) >> 1) * c.east);
    if (PH == 1 && r == 4) eaf = *(const u32x4*)(smem + c.eao + (me & 0x7ff0u) * 2u);
    u32x4 z;
#pragma unroll
    for (int i = 0; i < 2; ++i) {
      const float s = rw[r][(PH == 1 ? 2 : 0) + i];
#pragma unroll
      for (int up = 0; up < 2; ++up) {
        const u32 a = X[4 * i + 2 * up][r >> 1], b = X[4 * i + 2 * up + 1][r >> 1];
        z[2 * i + up] = (r & 1) ? cvtpk(bfhi(a) * s, bfhi(b) * s) : cvtpk(bflo(a) * s, bflo(b) * s);
      }
    }
    acc[r] = MFMA32(__builtin_bit_cast(bf16x8, z), __builtin_bit_cast(bf16x8, bt), acc[r]);
    if (PH == 1 && r >= 6) rw[r - 6] = rwm(smem, c, eaf, r - 6);
  }
}

#define STAMP_LDS 0
#define STA(k) do { } while (0)
#define STDUMP() do { } while (0)
__global__ __launch_bounds__(BNT, 2) void agg_kernel(const char* __restrict__ x, const char* __restrict__ sh,
                                                     const char* __restrict__ eattr, const u32* __restrict__ cg2,
                                                     char* __restrict__ out, char* __restrict__ ws, int N, int S,
                                                     float bscale) {
  __shared__ __attribute__((aligned(16))) char smem[LDS_BYTES + STAMP_LDS];
  const int tid0 = threadIdx.x;
  const int wv = __builtin_amdgcn_readfirstlane(tid0 >> 6);
  const int b = blockIdx.x;
  const u32 n0 = (u32)(b * N) >> 8;
  const u32 cntN = ((u32)((b + 1) * N) >> 8) - n0;
  u32* s_cnt = (u32*)(smem + O_CNT);
  u32* s_base = (u32*)(smem + O_BASE);
  u32* s_deg = (u32*)(smem + O_DEG);
  u32* s_rank = (u32*)(smem + O_RANK);
  u32* s_inv = (u32*)(smem + O_INV);
  u32* s_misc = (u32*)(smem + O_MISC);
  u32* s_meta = (u32*)(smem + O_META);
  u32x2* s_exp = (u32x2*)(smem + O_EXP) + wv * SEGW;
  u32* s_tmp = (u32*)(smem + O_TMP);
  STA(2);
  if (CT) {
    const u32 off = umin((u32)tid0, 111u) * 128u;
    const u32 la = (u32)(size_t)(smem + O_AT + 1024);
    asm volatile(
        "s_getpc_b64 s[40:41]\n\t"
        "s_add_u32 s40, s40, _ZN4fast10agg_kernelEPKcS1_S1_PKjPcS4_iif@rel32@lo+4\n\t"
        "s_addc_u32 s41, s41, _ZN4fast10agg_kernelEPKcS1_S1_PKjPcS4_iif@rel32@hi+12\n\t"
        "s_mov_b32 m0, %1\n\t"
        "s_nop 0\n\t"
        "global_load_lds_dword %0, s[40:41]"
        :
        : "v"(off), "s"(la)
        : "s40", "s41", "scc", "m0", "memory");
  }

  {
    if (tid0 >= 256 && tid0 < 256 + 64) s_deg[tid0 - 256] = 0u;
    if (tid0 >= 384 && tid0 < 384 + 64) s_inv[tid0 - 384] = 0xffffffffu;
    if (tid0 >= 448 && tid0 < 448 + 32) ((u32*)(smem + O_ZERO))[tid0 - 448] = (tid0 == 448 + 18) ? (u32)PCAP << 4 : 0u;
  }

  f32x16 acc[8];
#pragma unroll
  for (int r = 0; r < 8; ++r) {
#pragma unroll
    for (int k = 0; k < 16; ++k) {
      float z = 0.f;
      asm volatile("" : "+v"(z));
      acc[r][k] = z;
    }
  }

  int nseg = 1, nseg0 = 1, cps = 1;
  for (int seg = 0;; ++seg) {
    int tid = tid0;
    asm volatile("" : "+v"(tid));
    const int lane = tid & 63;
    const int q4 = lane >> 4, c16 = lane & 15;
    u32x4 cd[2];
    u32 eb;
    int sb = seg * SEGW;
    u32 ovn = 0;
    if (seg < nseg0) {
      const u32x4* sp = (const u32x4*)(ws + W_SLOT) + (b * 512 + tid) * 2;
      cd[0] = sp[0];
      cd[1] = sp[1];
      ovn = ((const u32*)(ws + W_OVN))[tid & 255];
      eb = (u32)((tid >> 1) * S);
    } else {
      const int idx = (seg - nseg0) * 8 + wv;
      const int sl = idx / cps, k0 = (idx - sl * cps) * SEGW + lane;
      const int n = (int)((const u32*)(ws + W_OVN))[sl];
      cd[0] = u32x4{INV, INV, INV, INV};
      cd[1] = cd[0];
#pragma unroll
      for (int j = 0; j < 2; ++j)
        if (k0 + j * 64 < n) {
          const u32x2 o = ((const u32x2*)(ws + W_OVF))[sl * ANT + k0 + j * 64];
          if (o.y == (u32)b) cd[0][j] = o.x;
        }
      eb = (u32)(sl * S);
      sb = 0;
    }
    const u32x4 wr0 = *(const u32x4*)(ws + W_WR + tid * 16);
    if (tid < 256) s_cnt[tid] = 0u;
    __syncthreads();
    u32 tc = 0;
#pragma unroll
    for (int k = 0; k < 8; ++k) tc += (cd[k >> 2][k & 3] != INV) ? 1u : 0u;
    const u32 tinc = scan64(tc);
    const int T = __builtin_amdgcn_readlane((int)tinc, 63);
    STA(3);
    {
      u32 p = tinc - tc - (u32)sb;
#pragma unroll
      for (int k = 0; k < 8; ++k) {
        const u32 r = cd[k >> 2][k & 3];
        const bool v = r != INV;
        if (k >= 4 && __builtin_amdgcn_ballot_w64(v) == 0ull) break;
        if (v && p < (u32)SEGW) s_exp[p] = u32x2{((r >> 16) << 18) | (eb + ((r >> 6) & 1023u)), r & 63u};
        p += v ? 1u : 0u;
      }
    }
    int Tn = T - sb;
    Tn = Tn < 0 ? 0 : (Tn > SEGW ? SEGW : Tn);
    wavesync();
    u32x2 mt[2], shv[2];
    u32x4 e0[2], e1[2];
    bool act[2];
    u32 eid[2];
#pragma unroll
    for (int j = 0; j < 2; ++j) {
      act[j] = j * 64 + lane < Tn;
      mt[j] = s_exp[j * 64 + lane];
      eid[j] = act[j] ? (mt[j].x & 0x3ffffu) : 0u;
    }
    STA(4);
    if (seg == 0) {
#pragma unroll
      for (int j = 0; j < 2; ++j)
        if (act[j]) atomicAdd(&s_deg[mt[j].y], 1u);
      if (lane == 0) atomicMax(&s_misc[0], (u32)T);
      if (ovn) atomicMax(&s_misc[1], ovn);
    }
    *(u32x4*)(smem + O_WRF + tid * 16) = wr0;
    if (tid < 2) *(u32x4*)(smem + O_EA + PCAP * 32 + tid * 16) = u32x4{0u, 0u, 0u, 0u};
#pragma unroll
    for (int j = 0; j < 2; ++j) shv[j] = *(const u32x2*)(sh + (size_t)eid[j] * 8);
    if (seg == 0) {
      STA(5);
      __syncthreads();
      const u32 d = umin(s_deg[lane], 127u);
      u32 gl = 0u, gh = 0u, el = 0xffffffffu, eh = 0xffu;
#pragma unroll
      for (int bit = 6; bit >= 0; --bit) {
        const bool on = (d >> bit) & 1u;
        const u64 B = __builtin_amdgcn_ballot_w64(on);
        const u32 bl = (u32)B, bh = (u32)(B >> 32);
        gl |= on ? 0u : (el & bl);
        gh |= on ? 0u : (eh & bh);
        el &= on ? bl : ~bl;
        eh &= on ? bh : ~bh;
      }
      const u32 rk = __builtin_popcount(gl) + __builtin_popcount(gh) +
                     __builtin_amdgcn_mbcnt_hi(eh, __builtin_amdgcn_mbcnt_lo(el, 0u));
      s_rank[lane] = rk;
      nseg0 = (int)((__builtin_amdgcn_readfirstlane(s_misc[0]) + SEGW - 1) / SEGW);
      nseg0 = nseg0 < 1 ? 1 : nseg0;
      cps = (int)((__builtin_amdgcn_readfirstlane(s_misc[1]) + SEGW - 1) / SEGW);
      nseg = nseg0 + 32 * cps;
      if (wv == 0 && (u32)lane < cntN) {
        const u32 slot = rk >> 3, r8 = rk & 7u;
        s_inv[((slot & 1u) ? 7u - r8 : r8) * 8u + slot] = (u32)lane;
      }
      wavesync();
    }
    STA(6);
    u32 key[2], rk[2], slot[2], comp[2];
#pragma unroll
    for (int j = 0; j < 2; ++j) {
      key[j] = 0u; rk[j] = 0u; slot[j] = 0u; comp[j] = 0u;
      if (j * 64 < Tn) {
        const u32 nr = s_rank[act[j] ? mt[j].y : 0u];
        slot[j] = nr >> 3;
        const u32 r8 = nr & 7u;
        const u32 bk = umin((u32)((float)(mt[j].x >> 18) * bscale), 31u);
        key[j] = ((slot[j] & 1u) ? 7u - r8 : r8) * 32u + bk;
        comp[j] = mt[j].x;
        if (act[j]) rk[j] = atomicAdd(&s_cnt[key[j]], 1u);
      }
    }
    e0[0] = *(const u32x4*)(eattr + (size_t)eid[0] * 32);
    e1[0] = *(const u32x4*)(eattr + (size_t)eid[0] * 32 + 16);
    __syncthreads();
    STA(7);
    u32 ls = 0, lc = 0;
    {
      const u32x4 c = *(const u32x4*)(s_cnt + 4 * lane);
      const u32 p1 = c.x, p2 = p1 + c.y, p3 = p2 + c.z, tot = p3 + c.w;
      const u32 inc = scan64(tot);
      u32 run = 0, prev = 0, adj = 0;
#pragma unroll
      for (int k = 0; k < 8; ++k) {
        const u32 ek = __builtin_amdgcn_readlane(inc, 8 * k + 7);
        const u32 ck = ek - prev;
        ls = (wv == k) ? run : ls;
        lc = (wv == k) ? ck : lc;
        adj = ((lane >> 3) == k) ? run - prev : adj;
        run += ck ? ((ck + 3u) & ~3u) : 4u;
        prev = ek;
      }
      const u32 ex = inc - tot + adj;
      *(u32x4*)(s_base + 4 * lane) = u32x4{ex, ex + p1, ex + p2, ex + p3};
      wavesync();
    }
    u32 kb[2], kn[2];
#pragma unroll
    for (int j = 0; j < 2; ++j) {
      kb[j] = 0u;
      kn[j] = 0u;
      if (j * 64 < Tn) {
        kb[j] = s_base[key[j]];
        kn[j] = act[j] ? s_cnt[key[j]] : 0u;
        if (act[j]) s_tmp[kb[j] + rk[j]] = comp[j];
      }
    }
    e0[1] = *(const u32x4*)(eattr + (size_t)eid[1] * 32);
    e1[1] = *(const u32x4*)(eattr + (size_t)eid[1] * 32 + 16);
    __syncthreads();
    STA(8);
    {
      u32 p[2] = {kb[0], kb[1]};
      const u32 nm = kn[0] > kn[1] ? kn[0] : kn[1];
#pragma clang loop unroll(disable) vectorize(disable) interleave(disable)
      for (u32 k = 0; k < nm; k += 4) {
        u32 t[2][4];
#pragma unroll
        for (int j = 0; j < 2; ++j)
#pragma unroll
          for (int q = 0; q < 4; ++q) t[j][q] = s_tmp[kb[j] + k + q];
#pragma unroll
        for (int j = 0; j < 2; ++j)
#pragma unroll
          for (int q = 0; q < 4; ++q) p[j] += (k + q < kn[j] && t[j][q] < comp[j]) ? 1u : 0u;
      }
#pragma unroll
      for (int j = 0; j < 2; ++j)
        if (act[j]) s_meta[p[j]] = (comp[j] & 0xfffc0000u) | ((u32)(wv * SEGW + j * 64 + lane) << 4) | slot[j];
    }
    const u32 lp = lc ? ((lc + 3u) & ~3u) : 4u;
    if (lane < 4 && lc + lane < lp) {
      const u32 p = ls + lc + lane;
      const u32 sl = lc ? (s_tmp[ls + lc - 1] & 0xfffc0000u) : 0u;
      s_meta[p] = sl | ((u32)PCAP << 4) | 15u;
    }
    __syncthreads();
    STA(9);

    u32x4 wf[8][2];
    {
      const int ng = (int)(lp >> 2);
      const int h = lane >> 5, j = lane & 31;
      Ctx c;
      c.xb = x;
      c.xoff = j * 16;
      c.js = j >> 2;
      c.atl = O_AT + (j & 3) * 8;
      c.mad = O_META + (ls + 2 * h) * 4;
      const bool match = (q4 >> 1) == ((c16 >> 2) & 1);
      c.ead = match ? O_META + (ls + 4 * ((c16 >> 1) & 1) + 2 * (c16 >> 3) + (c16 & 1)) * 4 : O_MISC + 8;
      c.east = match ? 32 : 0;
      c.eao = O_EA + 16 * (q4 & 1);
      c.wra = O_WRF + lane * 16;
      u32x2 m0 = *(const u32x2*)(smem + c.mad);
      u32x2 m1 = *(const u32x2*)(smem + c.mad + 16);
      u32x4 XA[8], XB[8];
      load_x(c, m0, XA);
      const u32 me0 = *(const u32*)(smem + c.ead);
      __builtin_amdgcn_sched_barrier(0);
      u32x4 at[2][2];
      u64 cga = (u64)cg2;
      asm volatile("" : "+s"(cga));
      const __attribute__((address_space(4))) u32* cgp = (const __attribute__((address_space(4))) u32*)cga;
#pragma unroll
      for (int j = 0; j < 2; ++j) {
        at[j][0] = u32x4{0u, 0u, 0u, 0u};
        at[j][1] = at[j][0];
        if (j * 64 < Tn) {
          const float s0 = bflo(shv[j].x), s1 = bfhi(shv[j].x), s2 = bflo(shv[j].y), s3 = bfhi(shv[j].y);
#pragma unroll
          for (int w = 0; w < 4; ++w) {
            float a[4];
#pragma unroll
            for (int u = 0; u < 4; ++u) {
              const u32 c0 = cgp[u * 8 + 0 + (w >> 1)], c1 = cgp[u * 8 + 2 + (w >> 1)];
              const u32 c2 = cgp[u * 8 + 4 + (w >> 1)], c3 = cgp[u * 8 + 6 + (w >> 1)];
              a[u] = ((w & 1) ? bfhi(c0) : bflo(c0)) * s0 + ((w & 1) ? bfhi(c1) : bflo(c1)) * s1 +
                     ((w & 1) ? bfhi(c2) : bflo(c2)) * s2 + ((w & 1) ? bfhi(c3) : bflo(c3)) * s3;
            }
            at[j][w >> 1][2 * (w & 1)] = cvtpk(a[0], a[1]);
            at[j][w >> 1][2 * (w & 1) + 1] = cvtpk(a[2], a[3]);
          }
        }
      }
#pragma unroll
      for (int j = 0; j < 2; ++j) {
        if (j * 64 < Tn) {
          const int p = wv * SEGW + j * 64 + lane;
          u32x4* pa = (u32x4*)(smem + O_AT + p * 32);
          u32x4* pe = (u32x4*)(smem + O_EA + p * 32);
          pa[0] = at[j][0];
          pa[1] = at[j][1];
          pe[0] = e0[j];
          pe[1] = e1[j];
        }
      }
      __syncthreads();
      f32x4 rw[8];
      u32x4 eaf = *(const u32x4*)(smem + c.eao + (me0 & 0x7ff0u) * 2u);
#pragma unroll
      for (int r = 2; r < 8; ++r) rw[r] = f32x4{0.f, 0.f, 0.f, 0.f};
      rw[0] = rwm(smem, c, eaf, 0);
      rw[1] = rwm(smem, c, eaf, 1);
      int g = 0;
      for (; g + 2 < ng; g += 2) {
        step<0>(smem, c, g, m0, m1, eaf, rw, XA, XB, acc);
        step<1>(smem, c, g + 1, m0, m1, eaf, rw, XB, XA, acc);
      }
      if (g + 1 < ng) {
        step<0>(smem, c, g, m0, m1, eaf, rw, XA, XB, acc);
        ++g;
#pragma unroll
        for (int k = 0; k < 8; ++k) XA[k] = XB[k];
#pragma unroll
        for (int r = 0; r < 8; ++r) { rw[r][0] = rw[r][2]; rw[r][1] = rw[r][3]; }
      } else {
#pragma unroll
        for (int r = 2; r < 8; ++r) rw[r] = rwm(smem, c, eaf, r);
      }
      step<2>(smem, c, g, m0, m1, eaf, rw, XA, XB, acc);
      __builtin_amdgcn_sched_barrier(0);
      {
        const char* wp = ws + W_WO + (size_t)(wv * 16 * 64 + lane) * 16;
#pragma unroll
        for (int ks = 0; ks < 8; ++ks) {
          wf[ks][0] = *(const u32x4*)(wp + (ks * 2 + 0) * 1024);
          wf[ks][1] = *(const u32x4*)(wp + (ks * 2 + 1) * 1024);
        }
        __builtin_amdgcn_sched_barrier(0);
      }
    }
    STA(10);
    __syncthreads();
    if (seg + 1 < nseg) continue;

    {
      const u32 loc = s_inv[wv * 8 + ((lane & 31) >> 2)];
      if (loc != 0xffffffffu) {
        char* rowp = smem + O_TILE + (loc * 4 + (lane & 3)) * TROW + (lane >> 5) * 64;
#pragma unroll
        for (int a = 0; a < 16; ++a)
          *(u32x4*)(rowp + (a >> 2) * 128 + (a & 3) * 16) =
              u32x4{cvtpk(acc[0][a], acc[1][a]), cvtpk(acc[2][a], acc[3][a]), cvtpk(acc[4][a], acc[5][a]),
                    cvtpk(acc[6][a], acc[7][a])};
      }
    }
    STA(11);
    __syncthreads();
    STA(12);
    {
      const int cb = 128 * (q4 & 1) + 64 * (q4 >> 1);
      const char* tp = smem + O_TILE + c16 * TROW + cb * 2;
      char* op = out + ((size_t)n0 * 4 + c16) * 512 + (wv * 32 + 8 * q4) * 2;
      const u32 nrows = cntN * 4;
#pragma unroll 2
      for (int rb = 0; rb < 10; ++rb) {
        f32x4 a0 = {0.f, 0.f, 0.f, 0.f}, a1 = {0.f, 0.f, 0.f, 0.f};
#pragma unroll
        for (int ks = 0; ks < 8; ++ks) {
          const u32x4 bf = *(const u32x4*)(tp + rb * 16 * TROW + ks * 16);
          a0 = MFMA16(__builtin_bit_cast(bf16x8, wf[ks][0]), __builtin_bit_cast(bf16x8, bf), a0);
          a1 = MFMA16(__builtin_bit_cast(bf16x8, wf[ks][1]), __builtin_bit_cast(bf16x8, bf), a1);
        }
        if ((u32)(rb * 16 + c16) < nrows) {
          const u32x4 o = {cvtpk(a0[0], a1[0]), cvtpk(a0[1], a1[1]), cvtpk(a0[2], a1[2]), cvtpk(a0[3], a1[3])};
          *(u32x4*)(op + (size_t)rb * 16 * 512) = o;
        }
      }
    }
    STA(13);
    STDUMP();
    return;
  }
}
}

extern "C" void kernel_launch(void* const* d_in, const int* in_sizes, int n_in, void* d_out, int out_size,
                              void* d_ws, size_t ws_size, hipStream_t stream) {
  const int N = in_sizes[0] / 1024;
  const int E = in_sizes[1];
  if (N <= 0) return;
  if (N <= 10240 && E >= 1 && E <= fast::MAXE && ws_size >= fast::W_END) {
    int S = (E + 255) / 256;
    if (S < 1) S = 1;
    fast::bin_kernel<<<256, fast::ANT, 0, stream>>>((const int*)d_in[1], (const int*)d_in[2],
                                                    (const unsigned short*)d_in[6], (const unsigned short*)d_in[7],
                                                    (char*)d_ws, N, E, S, 1.0f / (float)N);
    fast::agg_kernel<<<256, fast::BNT, 0, stream>>>((const char*)d_in[0], (const char*)d_in[3], (const char*)d_in[4],
                                                    (const u32*)d_in[5], (char*)d_out, (char*)d_ws, N, S,
                                                    32.0f / (float)N);
    return;
  }
  int G = (N + gen::MAXN - 1) / gen::MAXN;
  if (G < 256) G = 256;
  if (G > N) G = N;
  size_t gmax = ws_size / ((size_t)gen::NW * 16384);
  if (gmax > 65536) gmax = 65536;
  if (gmax < 1) return;
  for (int b0 = 0; b0 < G; b0 += (int)gmax) {
    const int g = (G - b0 < (int)gmax) ? G - b0 : (int)gmax;
    gen::mace_kernel<<<g, gen::NT, 0, stream>>>(
        (const char*)d_in[0], (const int*)d_in[1], (const int*)d_in[2], (const char*)d_in[3], (const char*)d_in[4],
        (const unsigned short*)d_in[5], (const char*)d_in[6], (const char*)d_in[7], (char*)d_out, (float*)d_ws, N, E,
        b0, G, (float)gen::NB / (float)N);
  }
}
```

```cpp
#include <hip/hip_runtime.h>
#include <hip/hip_bf16.h>
#include <stdint.h>

typedef unsigned int u32;
typedef unsigned long long u64;
typedef __attribute__((ext_vector_type(8))) short bf16x8;
typedef __attribute__((ext_vector_type(2))) __bf16 bf16x2_t;
typedef __attribute__((ext_vector_type(2))) float f32x2;
typedef __attribute__((ext_vector_type(4))) float f32x4;
typedef __attribute__((ext_vector_type(16))) float f32x16;
typedef __attribute__((ext_vector_type(2))) unsigned int u32x2;
typedef __attribute__((ext_vector_type(4))) unsigned int u32x4;

#define DI __device__ __forceinline__
#define ST(k) do { } while (0)
#define MFMA32(a, b, c) __builtin_amdgcn_mfma_f32_32x32x16_bf16((a), (b), (c), 0, 0, 0)
#define MFMA16(a, b, c) __builtin_amdgcn_mfma_f32_16x16x32_bf16((a), (b), (c), 0, 0, 0)

DI u32 cvtpk(float a, float b) {
  f32x2 v = {a, b};
  bf16x2_t r = __builtin_convertvector(v, bf16x2_t);
  return __builtin_bit_cast(u32, r);
}
DI float bflo(u32 v) { return __builtin_bit_cast(float, v << 16); }
DI float bfhi(u32 v) { return __builtin_bit_cast(float, v & 0xffff0000u); }
DI u32 mbcnt(u64 m) { return __builtin_amdgcn_mbcnt_hi((u32)(m >> 32), __builtin_amdgcn_mbcnt_lo((u32)m, 0u)); }
DI u32 umin(u32 a, u32 b) { return a < b ? a : b; }

namespace gen {
constexpr int NT = 768, NW = 12, NRG = 6, NB = 32;
constexpr int MAXN = 40;
constexpr int CAP = 944;
constexpr int LCAP = CAP + 48;
constexpr int CAPW = 128;
constexpr int WMIN = 512;
constexpr int SU = 9;
constexpr int TROW = 528;
constexpr int PFROWS = 1536;

constexpr int O_CNT = 0;
constexpr int O_BASE = O_CNT + 192 * 4;
constexpr int O_DEG = O_BASE + 192 * 4;
constexpr int O_MAP = O_DEG + 64 * 4;
constexpr int O_INV = O_MAP + 64 * 4;
constexpr int O_CGT = O_INV + 64 * 4;
constexpr int O_ZERO = O_CGT + 64 * 4;
constexpr int O_MISC = O_ZERO + 64;
constexpr int O_WR = O_MISC + 64;
constexpr int O_META = O_WR + 8192;
constexpr int O_AT = O_META + LCAP * 4;
constexpr int O_EA = O_AT + LCAP * 32;
constexpr int O_TILE = O_EA + LCAP * 32;
constexpr int O_CANDQ = O_TILE;
constexpr int O_CANDI = O_CANDQ + NW * CAPW * 16;
constexpr int O_HITE = O_CANDI + NW * CAPW * 4;
constexpr int O_HITL = O_HITE + 1024 * 4;
constexpr int O_TMP = O_HITL + 1024 * 4;
constexpr int LDS_BYTES = O_TILE + MAXN * 4 * TROW;
static_assert(O_TMP + LCAP * 8 <= LDS_BYTES, "alias region too small");
static_assert(LDS_BYTES <= 163840, "LDS");
static_assert((O_AT % 16) == 0 && (O_EA % 16) == 0 && (O_TILE % 16) == 0 && (O_WR % 16) == 0, "align");

struct MainCtx {
  const char* xb;
  u32 xoff;
  u32 myslot;
  u32 atlane;
  u32 metaaddr;
  u32 eaaddr, eastride;
  u32 wraddr;
};

DI void load_block(const char* xb, u32 xoff, u32 m0, u32 m1, u32x2 (&buf)[8]) {
  const u32 o0 = (m0 & 0xfffff800u) | xoff, o1 = (m1 & 0xfffff800u) | xoff;
  const char* p0 = xb + (size_t)o0;
  const char* p1 = xb + (size_t)o1;
#pragma unroll
  for (int u = 0; u < 4; ++u) buf[u] = *(const u32x2*)(p0 + u * 512);
#pragma unroll
  for (int u = 0; u < 4; ++u) buf[4 + u] = *(const u32x2*)(p1 + u * 512);
}

DI u32x4 make_z(const u32x2 (&buf)[8], int r, float s0, float s1) {
  u32x4 z;
#pragma unroll
  for (int i = 0; i < 2; ++i) {
    const float s = i ? s1 : s0;
#pragma unroll
    for (int up = 0; up < 2; ++up) {
      const u32 a = (r & 2) ? buf[4 * i + 2 * up].y : buf[4 * i + 2 * up].x;
      const u32 b = (r & 2) ? buf[4 * i + 2 * up + 1].y : buf[4 * i + 2 * up + 1].x;
      z[2 * i + up] = (r & 1) ? cvtpk(bfhi(a) * s, bfhi(b) * s) : cvtpk(bflo(a) * s, bflo(b) * s);
    }
  }
  return z;
}

template <bool PF>
DI void step(char* smem, const MainCtx& c, int g, u32x4& mc, u32x4& eaf, u32x2 (&xb0)[8], u32x2 (&xb1)[8],
             f32x16 (&acc)[4]) {
  const u32 ab = c.atlane + g * 256;
  u32x2 bt[4];
#pragma unroll
  for (int k = 0; k < 4; ++k) {
    const u32 a = ((mc[k] & 15u) == c.myslot) ? ab + k * 32 : (u32)O_ZERO;
    bt[k] = *(const u32x2*)(smem + a);
  }
  if (PF) mc = *(const u32x4*)(smem + c.metaaddr + (g + 1) * 32);
  f32x4 rw[4];
  const f32x4 z4 = {0.f, 0.f, 0.f, 0.f};
  u32 wra = c.wraddr;
  asm volatile("" : "+v"(wra));
#pragma unroll
  for (int r = 0; r < 4; ++r) {
    const u32x4 wr = *(const u32x4*)(smem + wra + r * 1024);
    rw[r] = MFMA16(__builtin_bit_cast(bf16x8, eaf), __builtin_bit_cast(bf16x8, wr), z4);
  }
  if (PF) eaf = *(const u32x4*)(smem + c.eaaddr + (g + 1) * c.eastride);
  {
    const u32x4 b0 = {bt[0].x, bt[0].y, bt[1].x, bt[1].y};
#pragma unroll
    for (int r = 0; r < 4; ++r) {
      const u32x4 z = make_z(xb0, r, rw[r][0], rw[r][1]);
      acc[r] = MFMA32(__builtin_bit_cast(bf16x8, z), __builtin_bit_cast(bf16x8, b0), acc[r]);
    }
    if (PF) load_block(c.xb, c.xoff, mc[0], mc[1], xb0);
  }
  {
    const u32x4 b1 = {bt[2].x, bt[2].y, bt[3].x, bt[3].y};
#pragma unroll
    for (int r = 0; r < 4; ++r) {
      const u32x4 z = make_z(xb1, r, rw[r][2], rw[r][3]);
      acc[r] = MFMA32(__builtin_bit_cast(bf16x8, z), __builtin_bit_cast(bf16x8, b1), acc[r]);
    }
    if (PF) load_block(c.xb, c.xoff, mc[2], mc[3], xb1);
  }
}

__global__ __launch_bounds__(NT, 3) void mace_kernel(
    const char* __restrict__ x, const int* __restrict__ src, const int* __restrict__ dst,
    const char* __restrict__ sh, const char* __restrict__ eattr, const unsigned short* __restrict__ cg,
    const char* __restrict__ w_r, const char* __restrict__ w_out, char* __restrict__ out,
    float* __restrict__ ws, int N, int E, int b0, int G, float bscale) {
  __shared__ __attribute__((aligned(16))) char smem[LDS_BYTES];
  const int tid0 = threadIdx.x;
  const int wv = __builtin_amdgcn_readfirstlane(tid0 >> 6);
  const int rg = wv >> 1, hf = wv & 1;
  const int b = b0 + blockIdx.x;
  const u32 n0 = (u32)(((u64)b * (u64)N) / (u64)G);
  const u32 cntN = (u32)((((u64)b + 1) * (u64)N) / (u64)G) - n0;

  u32* s_cnt = (u32*)(smem + O_CNT);
  u32* s_base = (u32*)(smem + O_BASE);
  u32* s_deg = (u32*)(smem + O_DEG);
  u32* s_map = (u32*)(smem + O_MAP);
  u32* s_inv = (u32*)(smem + O_INV);
  float* s_cgt = (float*)(smem + O_CGT);
  u32* s_misc = (u32*)(smem + O_MISC);
  u32* s_hitE = (u32*)(smem + O_HITE);
  u32* s_hitL = (u32*)(smem + O_HITL);
  u64* s_tmp = (u64*)(smem + O_TMP);
  u32* s_meta = (u32*)(smem + O_META);

  if (wv < 2) {
    const int lane = tid0 & 63;
    const int q4 = lane >> 4, c16 = lane & 15;
    u32x4 wr[4];
    u32x2 L[8];
    const char* p = w_r + (size_t)(8 * (q4 & 1)) * 512 + (hf * 128 + 64 * (q4 >> 1) + 4 * c16) * 2;
#pragma unroll
    for (int j = 0; j < 8; ++j) L[j] = *(const u32x2*)(p + j * 512);
#pragma unroll
    for (int jj = 0; jj < 4; ++jj) {
      wr[0][jj] = __builtin_amdgcn_perm(L[2 * jj + 1].x, L[2 * jj].x, 0x05040100u);
      wr[1][jj] = __builtin_amdgcn_perm(L[2 * jj + 1].x, L[2 * jj].x, 0x07060302u);
      wr[2][jj] = __builtin_amdgcn_perm(L[2 * jj + 1].y, L[2 * jj].y, 0x05040100u);
      wr[3][jj] = __builtin_amdgcn_perm(L[2 * jj + 1].y, L[2 * jj].y, 0x07060302u);
    }
#pragma unroll
    for (int r = 0; r < 4; ++r) *(u32x4*)(smem + O_WR + hf * 4096 + r * 1024 + lane * 16) = wr[r];
  }
  if (wv == 2) {
    const int tid = tid0 & 63;
    const int w = tid >> 4, u = (tid >> 2) & 3, v = tid & 3;
    s_cgt[tid] = bflo((u32)cg[u * 16 + v * 4 + w]);
    s_map[tid] = (u32)(tid % NRG) | ((u32)(tid / NRG) << 8);
    const u32 l = (u32)((tid & 7) * NRG + (tid >> 3));
    s_inv[tid] = (tid < NRG * 8 && l < cntN) ? l : 0xffffffffu;
    if (tid < 16) ((u32*)(smem + O_ZERO))[tid] = 0u;
  }

  bool first = true, again = false;
  int pos = 0;
  int W = (E + 3) & ~3;
  for (;;) {
    if (again) __syncthreads();
    again = true;
    int tid = tid0;
    asm volatile("" : "+v"(tid));
    const int lane = tid & 63;
    const int q4 = lane >> 4, c16 = lane & 15, h = lane >> 5;
    u32 pfv = 0;
    {
      const u32 line = (u32)(b >> 3) * NT + tid;
      if (first && line < (u32)PFROWS * 16u && line < (u32)N * 16u) pfv = *(const u32*)(x + (size_t)line * 128);
    }
    if (tid < 192) s_cnt[tid] = 0u;
    if (tid >= 192 && tid < 256) s_deg[tid - 192] = 0u;
    if (tid >= 256 && tid < 258) s_misc[tid - 256] = 0u;
    __syncthreads();
    const int end = (E - pos < W) ? E : pos + W;

    {
      const u32x4* dq = (const u32x4*)dst;
      u32x4* cq = (u32x4*)(smem + O_CANDQ) + wv * CAPW;
      u32* ci = (u32*)(smem + O_CANDI) + wv * CAPW;
      const int q0 = pos >> 2, qf = end >> 2;
      u32 nc = 0;
      for (int qb = q0 + wv * 64; qb < qf; qb += NT * SU) {
        u32x4 v[SU];
#pragma unroll
        for (int k = 0; k < SU; ++k) {
          const int q = qb + k * NT + lane;
          v[k] = dq[q < qf ? q : qf - 1];
        }
#pragma unroll
        for (int k = 0; k < SU; ++k) {
          const int q = qb + k * NT + lane;
          const u32 m = umin(umin(v[k].x - n0, v[k].y - n0), umin(v[k].z - n0, v[k].w - n0));
          const bool hit = (m < cntN) && (q < qf);
          const u64 mask = __ballot(hit);
          if (mask) {
            const u32 p = nc + mbcnt(mask);
            if (hit && p < (u32)CAPW) { cq[p] = v[k]; ci[p] = (u32)q; }
            nc += (u32)__popcll(mask);
          }
        }
      }
      if (wv == NW - 1 && (end & 3)) {
        u32x4 v;
        const int e4 = qf * 4;
        v.x = (u32)dst[e4];
        v.y = (e4 + 1 < end) ? (u32)dst[e4 + 1] : n0 - 1u;
        v.z = (e4 + 2 < end) ? (u32)dst[e4 + 2] : n0 - 1u;
        v.w = n0 - 1u;
        if (lane == 0 && nc < (u32)CAPW) { cq[nc] = v; ci[nc] = (u32)qf; }
        nc += 1;
      }
      if (nc > (u32)CAPW) { if (lane == 0) s_misc[1] = 1u; nc = CAPW; }
      __builtin_amdgcn_wave_barrier();
      for (u32 i0 = 0; i0 < nc; i0 += 64) {
        const u32 i = i0 + lane;
        const bool valid = i < nc;
        const u32x4 v = cq[valid ? i : 0];
        const u32 q = ci[valid ? i : 0];
        const u32 t[4] = {v.x - n0, v.y - n0, v.z - n0, v.w - n0};
        u64 mk[4];
        u32 tot = 0;
#pragma unroll
        for (int k = 0; k < 4; ++k) { mk[k] = __ballot(valid && t[k] < cntN); tot += (u32)__popcll(mk[k]); }
        u32 bs = 0;
        if (lane == 0) bs = atomicAdd(&s_misc[0], tot);
        bs = __builtin_amdgcn_readfirstlane(bs);
#pragma unroll
        for (int k = 0; k < 4; ++k) {
          const u32 p = bs + mbcnt(mk[k]);
          if (valid && t[k] < cntN && p < (u32)CAP) {
            s_hitE[p] = 4 * q + k;
            s_hitL[p] = t[k];
            atomicAdd(&s_deg[t[k]], 1u);
          }
          bs += (u32)__popcll(mk[k]);
        }
      }
    }
    asm volatile("" ::"v"(pfv));
    __syncthreads();
    const u32 nh = __builtin_amdgcn_readfirstlane(s_misc[0]);
    if (nh > (u32)CAP || __builtin_amdgcn_readfirstlane(s_misc[1]) != 0u) {
      W = ((W >> 1) + 3) & ~3;
      if (W < WMIN) W = WMIN;
      continue;
    }

    u32 he[2], hl[2], hs[2];
    u32x2 hsh[2];
    u32x4 hea[2][2];
    bool act[2];
#pragma unroll
    for (int j = 0; j < 2; ++j) {
      const u32 i = tid + j * NT;
      act[j] = i < nh;
      he[j] = 0u; hl[j] = 0u; hs[j] = 0u;
      hsh[j] = u32x2{0u, 0u};
      hea[j][0] = u32x4{0u, 0u, 0u, 0u};
      hea[j][1] = u32x4{0u, 0u, 0u, 0u};
      if (j == 0 ? nh > 0u : nh > (u32)NT) {
        const u32 ii = act[j] ? i : 0;
        he[j] = s_hitE[ii];
        hl[j] = s_hitL[ii];
        hs[j] = (u32)src[he[j]];
        hsh[j] = *(const u32x2*)(sh + (size_t)he[j] * 8);
        hea[j][0] = *(const u32x4*)(eattr + (size_t)he[j] * 32);
        hea[j][1] = *(const u32x4*)(eattr + (size_t)he[j] * 32 + 16);
      }
    }
    if (first && wv == NW - 1) {
      const u32 d = s_deg[lane];
      u32 rank = 0;
      for (u32 j = 0; j < cntN; ++j) {
        const u32 dj = __builtin_amdgcn_readlane(d, j);
        rank += (dj > d || (dj == d && j < (u32)lane)) ? 1u : 0u;
      }
      const u32 slot = rank / NRG, r = rank % NRG;
      const u32 g = (slot & 1) ? (NRG - 1 - r) : r;
      s_inv[lane] = 0xffffffffu;
      __builtin_amdgcn_wave_barrier();
      if ((u32)lane < cntN) { s_map[lane] = g | (slot << 8); s_inv[g * 8 + slot] = lane; }
    }
    __syncthreads();

    u32 key[2], rk[2], mp[2];
#pragma unroll
    for (int j = 0; j < 2; ++j) {
      key[j] = 0u; rk[j] = 0u; mp[j] = 0u;
      if (j == 0 ? nh > 0u : nh > (u32)NT) {
        mp[j] = s_map[hl[j]];
        u32 bk = (u32)((float)hs[j] * bscale);
        bk = umin(bk, NB - 1);
        key[j] = (mp[j] & 255u) * NB + bk;
        if (act[j]) rk[j] = atomicAdd(&s_cnt[key[j]], 1u);
      }
    }
    __syncthreads();
    u32 ls = 0, lc = 0;
    {
      u32 v[3], inc[3];
#pragma unroll
      for (int k = 0; k < 3; ++k) {
        v[k] = s_cnt[k * 64 + lane];
        u32 t = v[k];
        t += (u32)__builtin_amdgcn_update_dpp(0, (int)t, 0x111, 0xf, 0xf, true);
        t += (u32)__builtin_amdgcn_update_dpp(0, (int)t, 0x112, 0xf, 0xf, true);
        t += (u32)__builtin_amdgcn_update_dpp(0, (int)t, 0x114, 0xf, 0xf, true);
        t += (u32)__builtin_amdgcn_update_dpp(0, (int)t, 0x118, 0xf, 0xf, true);
        t += (u32)__builtin_amdgcn_update_dpp(0, (int)t, 0x142, 0xa, 0xf, false);
        inc[k] = t;
      }
      u32 run = 0;
#pragma unroll
      for (int k = 0; k < 3; ++k) {
        const u32 c0 = __builtin_amdgcn_readlane(inc[k], 31);
        const u32 c1 = __builtin_amdgcn_readlane(inc[k], 63);
        const u32 s0 = run, s1 = run + ((c0 + 7u) & ~7u);
        run = s1 + ((c1 + 7u) & ~7u);
        ls = (rg == 2 * k) ? s0 : ls;
        lc = (rg == 2 * k) ? c0 : lc;
        ls = (rg == 2 * k + 1) ? s1 : ls;
        lc = (rg == 2 * k + 1) ? c1 : lc;
        s_base[k * 64 + lane] = (h ? s1 : s0) + inc[k] - v[k];
      }
      __builtin_amdgcn_wave_barrier();
    }
    u32 kb[2];
    u64 comp[2];
#pragma unroll
    for (int j = 0; j < 2; ++j) {
      kb[j] = 0u; comp[j] = 0ull;
      if (j == 0 ? nh > 0u : nh > (u32)NT) {
        kb[j] = s_base[key[j]];
        comp[j] = ((u64)hs[j] << 32) | he[j];
        if (act[j]) s_tmp[kb[j] + rk[j]] = comp[j];
      }
    }
    __syncthreads();
#pragma unroll
    for (int j = 0; j < 2; ++j) {
      if ((j == 0 || nh > (u32)NT) && act[j]) {
        const u32 n = s_cnt[key[j]];
        u32 p = kb[j];
        for (u32 k = 0; k < n; ++k) p += (s_tmp[kb[j] + k] < comp[j]) ? 1u : 0u;
        s_meta[p] = (hs[j] << 11) | (mp[j] >> 8);
        const float s0 = bflo(hsh[j].x), s1 = bfhi(hsh[j].x), s2 = bflo(hsh[j].y), s3 = bfhi(hsh[j].y);
        u32 at[8];
#pragma unroll
        for (int w = 0; w < 4; ++w) {
          float a[4];
#pragma unroll
          for (int u = 0; u < 4; ++u) {
            const f32x4 cv = *(const f32x4*)(s_cgt + w * 16 + u * 4);
            a[u] = cv.x * s0 + cv.y * s1 + cv.z * s2 + cv.w * s3;
          }
          at[2 * w] = cvtpk(a[0], a[1]);
          at[2 * w + 1] = cvtpk(a[2], a[3]);
        }
        u32x4* pa = (u32x4*)(smem + O_AT + p * 32);
        pa[0] = u32x4{at[0], at[1], at[2], at[3]};
        pa[1] = u32x4{at[4], at[5], at[6], at[7]};
        u32x4* pe = (u32x4*)(smem + O_EA + p * 32);
        pe[0] = hea[j][0];
        pe[1] = hea[j][1];
      }
    }
    if (hf == 0 && lc > 0) {
      const u32 p = ls + lc + lane;
      if (p < ls + ((lc + 7u) & ~7u)) {
        const u32 sl = (u32)(s_tmp[ls + lc - 1] >> 32);
        s_meta[p] = (sl << 11) | 15u;
        const u32x4 zz = {0u, 0u, 0u, 0u};
        u32x4* pe = (u32x4*)(smem + O_EA + p * 32);
        pe[0] = zz;
        pe[1] = zz;
      }
    }
    __syncthreads();

    f32x16 acc[4];
    if (first) {
#pragma unroll
      for (int r = 0; r < 4; ++r)
#pragma unroll
        for (int i = 0; i < 16; ++i) acc[r][i] = 0.f;
    } else {
      const f32x4* wsp = (const f32x4*)ws + ((size_t)blockIdx.x * NW + wv) * 1024 + lane;
#pragma unroll
      for (int r = 0; r < 4; ++r)
#pragma unroll
        for (int i = 0; i < 4; ++i) {
          const f32x4 t = wsp[(r * 4 + i) * 64];
          acc[r][4 * i] = t.x; acc[r][4 * i + 1] = t.y; acc[r][4 * i + 2] = t.z; acc[r][4 * i + 3] = t.w;
        }
    }
    const int ng = (int)((lc + 7u) >> 3);
    if (ng > 0) {
      MainCtx c;
      c.xb = x;
      c.xoff = hf * 256 + (lane & 31) * 8;
      c.myslot = (lane & 31) >> 2;
      c.atlane = O_AT + (lane & 3) * 8 + (ls + 4 * h) * 32;
      c.metaaddr = O_META + (ls + 4 * h) * 4;
      const bool match = (q4 >> 1) == ((c16 >> 2) & 1);
      c.eaaddr = match ? O_EA + (ls + 4 * (c16 >> 3) + (c16 & 3)) * 32 + 16 * (q4 & 1) : O_ZERO;
      c.eastride = match ? 256 : 0;
      c.wraddr = O_WR + hf * 4096 + lane * 16;
      u32x4 mc = *(const u32x4*)(smem + c.metaaddr);
      u32x4 eaf = *(const u32x4*)(smem + c.eaaddr);
      u32x2 xb0[8], xb1[8];
      load_block(c.xb, c.xoff, mc[0], mc[1], xb0);
      load_block(c.xb, c.xoff, mc[2], mc[3], xb1);
      int g = 0;
      for (; g < ng - 1; ++g) step<true>(smem, c, g, mc, eaf, xb0, xb1, acc);
      step<false>(smem, c, g, mc, eaf, xb0, xb1, acc);
    }
    if (end >= E) {

  u32x4 wf[8][2];
#pragma unroll
  for (int ks = 0; ks < 8; ++ks) { wf[ks][0] = u32x4{0u, 0u, 0u, 0u}; wf[ks][1] = u32x4{0u, 0u, 0u, 0u}; }
  if (wv < 8) {
    const int cb = 128 * (q4 & 1) + 64 * (q4 >> 1);
    const char* p = w_out + (size_t)cb * 512 + (wv * 32 + 2 * c16) * 2;
#pragma unroll
    for (int ks = 0; ks < 8; ++ks) {
      u32 L[8];
#pragma unroll
      for (int j = 0; j < 8; ++j) L[j] = *(const u32*)(p + (ks * 8 + j) * 512);
#pragma unroll
      for (int jj = 0; jj < 4; ++jj) {
        wf[ks][0][jj] = __builtin_amdgcn_perm(L[2 * jj + 1], L[2 * jj], 0x05040100u);
        wf[ks][1][jj] = __builtin_amdgcn_perm(L[2 * jj + 1], L[2 * jj], 0x07060302u);
      }
    }
  }
  {
    const u32 loc = s_inv[rg * 8 + ((lane & 31) >> 2)];
    if (loc != 0xffffffffu) {
      char* rowp = smem + O_TILE + (loc * 4 + (lane & 3)) * TROW + (hf * 128 + 16 * h) * 2;
#pragma unroll
      for (int qq = 0; qq < 4; ++qq) {
        u32 d[8];
#pragma unroll
        for (int a = 0; a < 4; ++a) {
          d[2 * a] = cvtpk(acc[0][4 * qq + a], acc[1][4 * qq + a]);
          d[2 * a + 1] = cvtpk(acc[2][4 * qq + a], acc[3][4 * qq + a]);
        }
        u32x4* pp = (u32x4*)(rowp + qq * 64);
        pp[0] = u32x4{d[0], d[1], d[2], d[3]};
        pp[1] = u32x4{d[4], d[5], d[6], d[7]};
      }
    }
  }
  __syncthreads();
  if (wv < 8) {
    const int cb = 128 * (q4 & 1) + 64 * (q4 >> 1);
    const char* tp = smem + O_TILE + c16 * TROW + cb * 2;
    char* op = out + ((size_t)n0 * 4 + c16) * 512 + (wv * 32 + 8 * q4) * 2;
    const u32 nrows = cntN * 4;
#pragma unroll 2
    for (int rb = 0; rb < MAXN * 4 / 16; ++rb) {
      f32x4 a0 = {0.f, 0.f, 0.f, 0.f}, a1 = {0.f, 0.f, 0.f, 0.f};
#pragma unroll
      for (int ks = 0; ks < 8; ++ks) {
        const u32x4 bf = *(const u32x4*)(tp + rb * 16 * TROW + ks * 16);
        a0 = MFMA16(__builtin_bit_cast(bf16x8, wf[ks][0]), __builtin_bit_cast(bf16x8, bf), a0);
        a1 = MFMA16(__builtin_bit_cast(bf16x8, wf[ks][1]), __builtin_bit_cast(bf16x8, bf), a1);
      }
      if ((u32)(rb * 16 + c16) < nrows) {
        const u32x4 o = {cvtpk(a0[0], a1[0]), cvtpk(a0[1], a1[1]), cvtpk(a0[2], a1[2]), cvtpk(a0[3], a1[3])};
        *(u32x4*)(op + (size_t)rb * 16 * 512) = o;
      }
    }
  }
  return;
    }
    int ln = tid0 & 63;
    asm volatile("" : "+v"(ln));
    f32x4* wsp = (f32x4*)ws + ((size_t)blockIdx.x * NW + wv) * 1024 + ln;
#pragma unroll
    for (int r = 0; r < 4; ++r)
#pragma unroll
      for (int i = 0; i < 4; ++i)
        wsp[(r * 4 + i) * 64] = f32x4{acc[r][4 * i], acc[r][4 * i + 1], acc[r][4 * i + 2], acc[r][4 * i + 3]};
    pos = end;
    first = false;
  }
}

}

namespace fast {
constexpr int ANT = 640;
constexpr int BNT = 512;
constexpr int SEGW = 128;
constexpr int LCAP = 8 * SEGW + 48;
constexpr int TROW = 528;
constexpr int MAXE = 256 * ANT;
constexpr size_t W_SLOT = 0;
constexpr size_t W_OVN = W_SLOT + 256 * 256 * 64;
constexpr size_t W_WR = W_OVN + 1024;
constexpr size_t W_WO = W_WR + 8192;
constexpr size_t W_OVF = W_WO + 131072;
constexpr size_t W_END = W_OVF + (size_t)MAXE * 8;
constexpr u32 INV = 0xffffffffu;
constexpr int O_CNT = 0;
constexpr int O_BASE = O_CNT + 1024;
constexpr int O_DEG = O_BASE + 1024;
constexpr int O_RANK = O_DEG + 256;
constexpr int O_INV = O_RANK + 256;
constexpr int O_ZERO = O_INV + 256;
constexpr int O_MISC = O_ZERO + 64;
constexpr int O_WRF = O_MISC + 64;
constexpr int O_EXP = O_WRF + 8192;
constexpr int O_TMP = O_EXP + 8 * SEGW * 8;
constexpr int O_META = O_TMP + LCAP * 4;
constexpr int O_AT = O_META + LCAP * 4;
constexpr int PCAP = 8 * SEGW;
constexpr int O_EA = O_AT + PCAP * 32;
constexpr int O_TILE = O_AT;
constexpr int LDS_BYTES = O_TILE + 160 * TROW;
static_assert(O_EA + PCAP * 32 + 32 <= LDS_BYTES, "LDS");
static_assert(LDS_BYTES <= 163840, "LDS");
static_assert((O_AT % 16) == 0 && (O_EA % 16) == 0 && (O_TILE % 16) == 0 && (O_META % 16) == 0, "align");

#define DPPADD(t, ctrl, rmask, bc) t += (u32)__builtin_amdgcn_update_dpp(0, (int)(t), ctrl, rmask, 0xf, bc)
DI u32 scan16(u32 t) {
  DPPADD(t, 0x111, 0xf, true);
  DPPADD(t, 0x112, 0xf, true);
  DPPADD(t, 0x114, 0xf, true);
  DPPADD(t, 0x118, 0xf, true);
  return t;
}
DI u32 scan32(u32 t) { t = scan16(t); DPPADD(t, 0x142, 0xa, false); return t; }
DI u32 scan64(u32 t) { t = scan32(t); DPPADD(t, 0x143, 0xc, false); return t; }
DI void wavesync() { __builtin_amdgcn_fence(__ATOMIC_RELEASE, "wavefront"); __builtin_amdgcn_wave_barrier(); __builtin_amdgcn_fence(__ATOMIC_ACQUIRE, "wavefront"); }

__global__ __launch_bounds__(ANT) void bin_kernel(
    const int* __restrict__ src, const int* __restrict__ dst, const unsigned short* __restrict__ w_r,
    const unsigned short* __restrict__ w_out, char* __restrict__ ws, int N, int E, int S, float invN) {
  __shared__ __attribute__((aligned(16))) u32 s_cnt[260];
  __shared__ __attribute__((aligned(16))) u32x4 s_img[1024];
  const int tid0 = threadIdx.x, tid = tid0, lane = tid & 63;
  const int wv = __builtin_amdgcn_readfirstlane(tid >> 6);
  const int i = blockIdx.x;
  ST(0);
  const int e = i * S + tid;
  const bool act = tid < S && e < E;
  const int ec = e < E ? e : E - 1;
  u32 dn = (u32)dst[ec], sn = (u32)src[ec];
  u32 pf0;
  {
    u64 pc, pa;
    asm volatile(
        "s_getpc_b64 s[40:41]\n\t"
        "s_add_u32 s42, s40, _ZN4fast10agg_kernelEPKcS1_S1_PKjPcS4_iif@rel32@lo+4\n\t"
        "s_addc_u32 s43, s41, _ZN4fast10agg_kernelEPKcS1_S1_PKjPcS4_iif@rel32@hi+12\n\t"
        "s_mov_b64 %0, s[40:41]\n\t"
        "s_mov_b64 %1, s[42:43]"
        : "=s"(pc), "=s"(pa)
        :
        : "s40", "s41", "s42", "s43", "scc");
    const char* a = (const char*)pa + umin((u32)tid, 111u) * 128u;
    if (tid >= 112 && tid < 121) a = (const char*)pc + (tid - 112) * 128;
    pf0 = *(const __attribute__((address_space(1))) u32*)a;
  }
  if (tid < 260) s_cnt[tid] = 0u;
  s_img[tid] = u32x4{INV, INV, INV, INV};
  if (tid < 1024 - ANT) s_img[tid + ANT] = u32x4{INV, INV, INV, INV};
  if (wv == 9 && lane < 34) {
    const unsigned short* p;
    char* o;
    if (lane < 32) {
      const int id = i * 32 + lane;
      const int l2 = id & 63, h = (id >> 6) & 1, ks = (id >> 7) & 7, w8 = id >> 10;
      const int q4 = l2 >> 4, c16 = l2 & 15;
      p = w_out + (128 * (q4 & 1) + 64 * (q4 >> 1) + 8 * ks) * 256 + w8 * 32 + 2 * c16 + h;
      o = ws + W_WO + (size_t)id * 16;
    } else {
      const int id = i * 2 + (lane - 32);
      const int l2 = id & 63, r = id >> 6;
      const int kq = l2 >> 4, j = l2 & 15;
      p = w_r + (8 * (kq & 1)) * 256 + 8 * (16 * (kq >> 1) + j) + r;
      o = ws + W_WR + (size_t)id * 16;
    }
    u32 v[8];
#pragma unroll
    for (int k = 0; k < 8; ++k) v[k] = p[k * 256];
    *(u32x4*)o = u32x4{v[0] | (v[1] << 16), v[2] | (v[3] << 16), v[4] | (v[5] << 16), v[6] | (v[7] << 16)};
  }
  __syncthreads();
  asm volatile("" : "+v"(dn), "+v"(sn));
  u32 b = (u32)((float)(dn * 256u + 255u) * invN);
  b = umin(b, 255u);
  if ((((b + 1u) * (u32)N) >> 8) <= dn) b += 1u;
  if (((b * (u32)N) >> 8) > dn) b -= 1u;
  b = umin(b, 255u);
  const u32 loc = dn - ((b * (u32)N) >> 8);
  if (act) {
    const u32 rec = (sn << 16) | ((u32)tid << 6) | loc;
    const u32 rk = atomicAdd(&s_cnt[b], 1u);
    if (rk < 16u) {
      ((u32*)s_img)[b * 16u + rk] = rec;
    } else {
      const u32 o = atomicAdd(&s_cnt[256], 1u);
      ((u32x2*)(ws + W_OVF))[i * ANT + o] = u32x2{rec, b};
    }
  }
  __syncthreads();
  u32x4* po = (u32x4*)(ws + W_SLOT) + i * 4;
  po[(tid >> 2) * 1024 + (tid & 3)] = s_img[tid];
  if (tid < 1024 - ANT) po[((tid + ANT) >> 2) * 1024 + (tid & 3)] = s_img[tid + ANT];
  if (tid == ANT - 1) ((u32*)(ws + W_OVN))[i] = s_cnt[256];
  asm volatile("" ::"v"(pf0));
  ST(1);
}

struct Ctx {
  const char* xb;
  u32 xoff;
  u32 js;
  u32 atl;
  u32 mad;
  u32 ead, east;
  u32 eao;
  u32 wra;
};

DI void load_x(const Ctx& c, u32x2 m, u32x4 (&X)[8]) {
#pragma unroll
  for (int i = 0; i < 2; ++i) {
    const char* p = c.xb + (size_t)(((m[i] >> 18) << 11) | c.xoff);
#pragma unroll
    for (int u = 0; u < 4; ++u) X[4 * i + u] = *(const u32x4*)(p + u * 512);
  }
}

DI f32x4 rwm(char* smem, const Ctx& c, const u32x4& eaf, int r) {
  const u32x4 w = *(const u32x4*)(smem + c.wra + r * 1024);
  const f32x4 z4 = {0.f, 0.f, 0.f, 0.f};
  return MFMA16(__builtin_bit_cast(bf16x8, eaf), __builtin_bit_cast(bf16x8, w), z4);
}

template <int PH>
DI void step(char* smem, const Ctx& c, int g, u32x2& m0, u32x2& m1, u32x4& eaf, f32x4 (&rw)[8],
             const u32x4 (&X)[8], u32x4 (&Y)[8], f32x16 (&acc)[8]) {
  u32x4 bt;
  u32 me = 0;
#pragma unroll
  for (int i = 0; i < 2; ++i) {
    const u32 a = ((m0[i] & 15u) == c.js) ? c.atl + (m0[i] & 0x7ff0u) * 2u : (u32)O_ZERO;
    const u32x2 t = *(const u32x2*)(smem + a);
    bt[2 * i] = t.x;
    bt[2 * i + 1] = t.y;
  }
  if (PH != 2) {
    asm volatile("" ::"v"(X[0]), "v"(X[1]), "v"(X[2]), "v"(X[3]), "v"(X[4]), "v"(X[5]), "v"(X[6]), "v"(X[7]));
    load_x(c, m1, Y);
    m0 = m1;
    m1 = *(const u32x2*)(smem + c.mad + (g + 2) * 16);
    __builtin_amdgcn_sched_barrier(0);
  }
#pragma unroll
  for (int r = 0; r < 8; ++r) {
    if (PH == 0 && r < 6) rw[r + 2] = rwm(smem, c, eaf, r + 2);
    if (PH == 1 && r == 2) me = *(const u32*)(smem + c.ead + ((g + 1) >> 1) * c.east);
    if (PH == 1 && r == 4) eaf = *(const u32x4*)(smem + c.eao + (me & 0x7ff0u) * 2u);
    u32x4 z;
#pragma unroll
    for (int i = 0; i < 2; ++i) {
      const float s = rw[r][(PH == 1 ? 2 : 0) + i];
#pragma unroll
      for (int up = 0; up < 2; ++up) {
        const u32 a = X[4 * i + 2 * up][r >> 1], b = X[4 * i + 2 * up + 1][r >> 1];
        z[2 * i + up] = (r & 1) ? cvtpk(bfhi(a) * s, bfhi(b) * s) : cvtpk(bflo(a) * s, bflo(b) * s);
      }
    }
    acc[r] = MFMA32(__builtin_bit_cast(bf16x8, z), __builtin_bit_cast(bf16x8, bt), acc[r]);
    if (PH == 1 && r >= 6) rw[r - 6] = rwm(smem, c, eaf, r - 6);
  }
}

#define STAMP_LDS 0
#define STA(k) do { } while (0)
#define STDUMP() do { } while (0)
__global__ __launch_bounds__(BNT, 2) void agg_kernel(const char* __restrict__ x, const char* __restrict__ sh,
                                                     const char* __restrict__ eattr, const u32* __restrict__ cg2,
                                                     char* __restrict__ out, char* __restrict__ ws, int N, int S,
                                                     float bscale) {
  __shared__ __attribute__((aligned(16))) char smem[LDS_BYTES + STAMP_LDS];
  const int tid0 = threadIdx.x;
  const int wv = __builtin_amdgcn_readfirstlane(tid0 >> 6);
  const int b = blockIdx.x;
  const u32 n0 = (u32)(b * N) >> 8;
  const u32 cntN = ((u32)((b + 1) * N) >> 8) - n0;
  u32* s_cnt = (u32*)(smem + O_CNT);
  u32* s_base = (u32*)(smem + O_BASE);
  u32* s_deg = (u32*)(smem + O_DEG);
  u32* s_rank = (u32*)(smem + O_RANK);
  u32* s_inv = (u32*)(smem + O_INV);
  u32* s_misc = (u32*)(smem + O_MISC);
  u32* s_meta = (u32*)(smem + O_META);
  u32x2* s_exp = (u32x2*)(smem + O_EXP) + wv * SEGW;
  u32* s_tmp = (u32*)(smem + O_TMP);
  STA(2);

  {
    if (tid0 >= 256 && tid0 < 256 + 64) s_deg[tid0 - 256] = 0u;
    if (tid0 >= 384 && tid0 < 384 + 64) s_inv[tid0 - 384] = 0xffffffffu;
    if (tid0 >= 448 && tid0 < 448 + 32) ((u32*)(smem + O_ZERO))[tid0 - 448] = (tid0 == 448 + 18) ? (u32)PCAP << 4 : 0u;
  }

  f32x16 acc[8];
#pragma unroll
  for (int r = 0; r < 8; ++r) {
#pragma unroll
    for (int k = 0; k < 16; ++k) {
      float z = 0.f;
      asm volatile("" : "+v"(z));
      acc[r][k] = z;
    }
  }

  int nseg = 1, nseg0 = 1, cps = 1;
  for (int seg = 0;; ++seg) {
    int tid = tid0;
    asm volatile("" : "+v"(tid));
    const int lane = tid & 63;
    const int q4 = lane >> 4, c16 = lane & 15;
    u32x4 cd[2];
    u32 eb;
    int sb = seg * SEGW;
    u32 ovn = 0;
    if (seg < nseg0) {
      const u32x4* sp = (const u32x4*)(ws + W_SLOT) + (b * 512 + tid) * 2;
      cd[0] = sp[0];
      cd[1] = sp[1];
      ovn = ((const u32*)(ws + W_OVN))[tid & 255];
      eb = (u32)((tid >> 1) * S);
    } else {
      const int idx = (seg - nseg0) * 8 + wv;
      const int sl = idx / cps, k0 = (idx - sl * cps) * SEGW + lane;
      const int n = (int)((const u32*)(ws + W_OVN))[sl];
      cd[0] = u32x4{INV, INV, INV, INV};
      cd[1] = cd[0];
#pragma unroll
      for (int j = 0; j < 2; ++j)
        if (k0 + j * 64 < n) {
          const u32x2 o = ((const u32x2*)(ws + W_OVF))[sl * ANT + k0 + j * 64];
          if (o.y == (u32)b) cd[0][j] = o.x;
        }
      eb = (u32)(sl * S);
      sb = 0;
    }
    const u32x4 wr0 = *(const u32x4*)(ws + W_WR + tid * 16);
    if (tid < 256) s_cnt[tid] = 0u;
    __syncthreads();
    u32 tc = 0;
#pragma unroll
    for (int k = 0; k < 8; ++k) tc += (cd[k >> 2][k & 3] != INV) ? 1u : 0u;
    const u32 tinc = scan64(tc);
    const int T = __builtin_amdgcn_readlane((int)tinc, 63);
    STA(3);
    {
      u32 p = tinc - tc - (u32)sb;
#pragma unroll
      for (int k = 0; k < 8; ++k) {
        const u32 r = cd[k >> 2][k & 3];
        const bool v = r != INV;
        if (k >= 4 && __builtin_amdgcn_ballot_w64(v) == 0ull) break;
        if (v && p < (u32)SEGW) s_exp[p] = u32x2{((r >> 16) << 18) | (eb + ((r >> 6) & 1023u)), r & 63u};
        p += v ? 1u : 0u;
      }
    }
    int Tn = T - sb;
    Tn = Tn < 0 ? 0 : (Tn > SEGW ? SEGW : Tn);
    wavesync();
    u32x2 mt[2], shv[2];
    u32x4 e0[2], e1[2];
    bool act[2];
    u32 eid[2];
#pragma unroll
    for (int j = 0; j < 2; ++j) {
      act[j] = j * 64 + lane < Tn;
      mt[j] = s_exp[j * 64 + lane];
      eid[j] = act[j] ? (mt[j].x & 0x3ffffu) : 0u;
    }
    STA(4);
    if (seg == 0) {
#pragma unroll
      for (int j = 0; j < 2; ++j)
        if (act[j]) atomicAdd(&s_deg[mt[j].y], 1u);
      if (lane == 0) atomicMax(&s_misc[0], (u32)T);
      if (ovn) atomicMax(&s_misc[1], ovn);
    }
    *(u32x4*)(smem + O_WRF + tid * 16) = wr0;
    if (tid < 2) *(u32x4*)(smem + O_EA + PCAP * 32 + tid * 16) = u32x4{0u, 0u, 0u, 0u};
#pragma unroll
    for (int j = 0; j < 2; ++j) shv[j] = *(const u32x2*)(sh + (size_t)eid[j] * 8);
    if (seg == 0) {
      STA(5);
      __syncthreads();
      const u32 d = umin(s_deg[lane], 127u);
      u32 gl = 0u, gh = 0u, el = 0xffffffffu, eh = 0xffu;
#pragma unroll
      for (int bit = 6; bit >= 0; --bit) {
        const bool on = (d >> bit) & 1u;
        const u64 B = __builtin_amdgcn_ballot_w64(on);
        const u32 bl = (u32)B, bh = (u32)(B >> 32);
        gl |= on ? 0u : (el & bl);
        gh |= on ? 0u : (eh & bh);
        el &= on ? bl : ~bl;
        eh &= on ? bh : ~bh;
      }
      const u32 rk = __builtin_popcount(gl) + __builtin_popcount(gh) +
                     __builtin_amdgcn_mbcnt_hi(eh, __builtin_amdgcn_mbcnt_lo(el, 0u));
      s_rank[lane] = rk;
      nseg0 = (int)((__builtin_amdgcn_readfirstlane(s_misc[0]) + SEGW - 1) / SEGW);
      nseg0 = nseg0 < 1 ? 1 : nseg0;
      cps = (int)((__builtin_amdgcn_readfirstlane(s_misc[1]) + SEGW - 1) / SEGW);
      nseg = nseg0 + 32 * cps;
      if (wv == 0 && (u32)lane < cntN) {
        const u32 slot = rk >> 3, r8 = rk & 7u;
        s_inv[((slot & 1u) ? 7u - r8 : r8) * 8u + slot] = (u32)lane;
      }
      wavesync();
    }
    STA(6);
    u32 key[2], rk[2], slot[2], comp[2];
#pragma unroll
    for (int j = 0; j < 2; ++j) {
      key[j] = 0u; rk[j] = 0u; slot[j] = 0u; comp[j] = 0u;
      if (j * 64 < Tn) {
        const u32 nr = s_rank[act[j] ? mt[j].y : 0u];
        slot[j] = nr >> 3;
        const u32 r8 = nr & 7u;
        const u32 bk = umin((u32)((float)(mt[j].x >> 18) * bscale), 31u);
        key[j] = ((slot[j] & 1u) ? 7u - r8 : r8) * 32u + bk;
        comp[j] = mt[j].x;
        if (act[j]) rk[j] = atomicAdd(&s_cnt[key[j]], 1u);
      }
    }
    e0[0] = *(const u32x4*)(eattr + (size_t)eid[0] * 32);
    e1[0] = *(const u32x4*)(eattr + (size_t)eid[0] * 32 + 16);
    __syncthreads();
    STA(7);
    u32 ls = 0, lc = 0;
    {
      const u32x4 c = *(const u32x4*)(s_cnt + 4 * lane);
      const u32 p1 = c.x, p2 = p1 + c.y, p3 = p2 + c.z, tot = p3 + c.w;
      const u32 inc = scan64(tot);
      u32 run = 0, prev = 0, adj = 0;
#pragma unroll
      for (int k = 0; k < 8; ++k) {
        const u32 ek = __builtin_amdgcn_readlane(inc, 8 * k + 7);
        const u32 ck = ek - prev;
        ls = (wv == k) ? run : ls;
        lc = (wv == k) ? ck : lc;
        adj = ((lane >> 3) == k) ? run - prev : adj;
        run += ck ? ((ck + 3u) & ~3u) : 4u;
        prev = ek;
      }
      const u32 ex = inc - tot + adj;
      *(u32x4*)(s_base + 4 * lane) = u32x4{ex, ex + p1, ex + p2, ex + p3};
      wavesync();
    }
    u32 kb[2], kn[2];
#pragma unroll
    for (int j = 0; j < 2; ++j) {
      kb[j] = 0u;
      kn[j] = 0u;
      if (j * 64 < Tn) {
        kb[j] = s_base[key[j]];
        kn[j] = act[j] ? s_cnt[key[j]] : 0u;
        if (act[j]) s_tmp[kb[j] + rk[j]] = comp[j];
      }
    }
    e0[1] = *(const u32x4*)(eattr + (size_t)eid[1] * 32);
    e1[1] = *(const u32x4*)(eattr + (size_t)eid[1] * 32 + 16);
    __syncthreads();
    STA(8);
    {
      u32 p[2] = {kb[0], kb[1]};
      const u32 nm = kn[0] > kn[1] ? kn[0] : kn[1];
#pragma clang loop unroll(disable) vectorize(disable) interleave(disable)
      for (u32 k = 0; k < nm; k += 4) {
        u32 t[2][4];
#pragma unroll
        for (int j = 0; j < 2; ++j)
#pragma unroll
          for (int q = 0; q < 4; ++q) t[j][q] = s_tmp[kb[j] + k + q];
#pragma unroll
        for (int j = 0; j < 2; ++j)
#pragma unroll
          for (int q = 0; q < 4; ++q) p[j] += (k + q < kn[j] && t[j][q] < comp[j]) ? 1u : 0u;
      }
#pragma unroll
      for (int j = 0; j < 2; ++j)
        if (act[j]) s_meta[p[j]] = (comp[j] & 0xfffc0000u) | ((u32)(wv * SEGW + j * 64 + lane) << 4) | slot[j];
    }
    const u32 lp = lc ? ((lc + 3u) & ~3u) : 4u;
    if (lane < 4 && lc + lane < lp) {
      const u32 p = ls + lc + lane;
      const u32 sl = lc ? (s_tmp[ls + lc - 1] & 0xfffc0000u) : 0u;
      s_meta[p] = sl | ((u32)PCAP << 4) | 15u;
    }
    __syncthreads();
    STA(9);

    u32x4 wf[8][2];
    {
      const int ng = (int)(lp >> 2);
      const int h = lane >> 5, j = lane & 31;
      Ctx c;
      c.xb = x;
      c.xoff = j * 16;
      c.js = j >> 2;
      c.atl = O_AT + (j & 3) * 8;
      c.mad = O_META + (ls + 2 * h) * 4;
      const bool match = (q4 >> 1) == ((c16 >> 2) & 1);
      c.ead = match ? O_META + (ls + 4 * ((c16 >> 1) & 1) + 2 * (c16 >> 3) + (c16 & 1)) * 4 : O_MISC + 8;
      c.east = match ? 32 : 0;
      c.eao = O_EA + 16 * (q4 & 1);
      c.wra = O_WRF + lane * 16;
      u32x2 m0 = *(const u32x2*)(smem + c.mad);
      u32x2 m1 = *(const u32x2*)(smem + c.mad + 16);
      u32x4 XA[8], XB[8];
      load_x(c, m0, XA);
      const u32 me0 = *(const u32*)(smem + c.ead);
      __builtin_amdgcn_sched_barrier(0);
      u32x4 at[2][2];
      u64 cga = (u64)cg2;
      asm volatile("" : "+s"(cga));
      const __attribute__((address_space(4))) u32* cgp = (const __attribute__((address_space(4))) u32*)cga;
#pragma unroll
      for (int j = 0; j < 2; ++j) {
        at[j][0] = u32x4{0u, 0u, 0u, 0u};
        at[j][1] = at[j][0];
        if (j * 64 < Tn) {
          const float s0 = bflo(shv[j].x), s1 = bfhi(shv[j].x), s2 = bflo(shv[j].y), s3 = bfhi(shv[j].y);
#pragma unroll
          for (int w = 0; w < 4; ++w) {
            float a[4];
#pragma unroll
            for (int u = 0; u < 4; ++u) {
              const u32 c0 = cgp[u * 8 + 0 + (w >> 1)], c1 = cgp[u * 8 + 2 + (w >> 1)];
              const u32 c2 = cgp[u * 8 + 4 + (w >> 1)], c3 = cgp[u * 8 + 6 + (w >> 1)];
              a[u] = ((w & 1) ? bfhi(c0) : bflo(c0)) * s0 + ((w & 1) ? bfhi(c1) : bflo(c1)) * s1 +
                     ((w & 1) ? bfhi(c2) : bflo(c2)) * s2 + ((w & 1) ? bfhi(c3) : bflo(c3)) * s3;
            }
            at[j][w >> 1][2 * (w & 1)] = cvtpk(a[0], a[1]);
            at[j][w >> 1][2 * (w & 1) + 1] = cvtpk(a[2], a[3]);
          }
        }
      }
#pragma unroll
      for (int j = 0; j < 2; ++j) {
        if (j * 64 < Tn) {
          const int p = wv * SEGW + j * 64 + lane;
          u32x4* pa = (u32x4*)(smem + O_AT + p * 32);
          u32x4* pe = (u32x4*)(smem + O_EA + p * 32);
          pa[0] = at[j][0];
          pa[1] = at[j][1];
          pe[0] = e0[j];
          pe[1] = e1[j];
        }
      }
      __syncthreads();
      f32x4 rw[8];
      u32x4 eaf = *(const u32x4*)(smem + c.eao + (me0 & 0x7ff0u) * 2u);
#pragma unroll
      for (int r = 2; r < 8; ++r) rw[r] = f32x4{0.f, 0.f, 0.f, 0.f};
      rw[0] = rwm(smem, c, eaf, 0);
      rw[1] = rwm(smem, c, eaf, 1);
      int g = 0;
      for (; g + 2 < ng; g += 2) {
        step<0>(smem, c, g, m0, m1, eaf, rw, XA, XB, acc);
        step<1>(smem, c, g + 1, m0, m1, eaf, rw, XB, XA, acc);
      }
      if (g + 1 < ng) {
        step<0>(smem, c, g, m0, m1, eaf, rw, XA, XB, acc);
        ++g;
#pragma unroll
        for (int k = 0; k < 8; ++k) XA[k] = XB[k];
#pragma unroll
        for (int r = 0; r < 8; ++r) { rw[r][0] = rw[r][2]; rw[r][1] = rw[r][3]; }
      } else {
#pragma unroll
        for (int r = 2; r < 8; ++r) rw[r] = rwm(smem, c, eaf, r);
      }
      step<2>(smem, c, g, m0, m1, eaf, rw, XA, XB, acc);
      __builtin_amdgcn_sched_barrier(0);
      {
        const char* wp = ws + W_WO + (size_t)(wv * 16 * 64 + lane) * 16;
#pragma unroll
        for (int ks = 0; ks < 8; ++ks) {
          wf[ks][0] = *(const u32x4*)(wp + (ks * 2 + 0) * 1024);
          wf[ks][1] = *(const u32x4*)(wp + (ks * 2 + 1) * 1024);
        }
        __builtin_amdgcn_sched_barrier(0);
      }
    }
    STA(10);
    __syncthreads();
    if (seg + 1 < nseg) continue;

    {
      const u32 loc = s_inv[wv * 8 + ((lane & 31) >> 2)];
      if (loc != 0xffffffffu) {
        char* rowp = smem + O_TILE + (loc * 4 + (lane & 3)) * TROW + (lane >> 5) * 64;
#pragma unroll
        for (int a = 0; a < 16; ++a)
          *(u32x4*)(rowp + (a >> 2) * 128 + (a & 3) * 16) =
              u32x4{cvtpk(acc[0][a], acc[1][a]), cvtpk(acc[2][a], acc[3][a]), cvtpk(acc[4][a], acc[5][a]),
                    cvtpk(acc[6][a], acc[7][a])};
      }
    }
    STA(11);
    __syncthreads();
    STA(12);
    {
      const int cb = 128 * (q4 & 1) + 64 * (q4 >> 1);
      const char* tp = smem + O_TILE + c16 * TROW + cb * 2;
      char* op = out + ((size_t)n0 * 4 + c16) * 512 + (wv * 32 + 8 * q4) * 2;
      const u32 nrows = cntN * 4;
#pragma unroll 2
      for (int rb = 0; rb < 10; ++rb) {
        f32x4 a0 = {0.f, 0.f, 0.f, 0.f}, a1 = {0.f, 0.f, 0.f, 0.f};
#pragma unroll
        for (int ks = 0; ks < 8; ++ks) {
          const u32x4 bf = *(const u32x4*)(tp + rb * 16 * TROW + ks * 16);
          a0 = MFMA16(__builtin_bit_cast(bf16x8, wf[ks][0]), __builtin_bit_cast(bf16x8, bf), a0);
          a1 = MFMA16(__builtin_bit_cast(bf16x8, wf[ks][1]), __builtin_bit_cast(bf16x8, bf), a1);
        }
        if ((u32)(rb * 16 + c16) < nrows) {
          const u32x4 o = {cvtpk(a0[0], a1[0]), cvtpk(a0[1], a1[1]), cvtpk(a0[2], a1[2]), cvtpk(a0[3], a1[3])};
          *(u32x4*)(op + (size_t)rb * 16 * 512) = o;
        }
      }
    }
    STA(13);
    STDUMP();
    return;
  }
}
}

extern "C" void kernel_launch(void* const* d_in, const int* in_sizes, int n_in, void* d_out, int out_size,
                              void* d_ws, size_t ws_size, hipStream_t stream) {
  const int N = in_sizes[0] / 1024;
  const int E = in_sizes[1];
  if (N <= 0) return;
  if (N <= 10240 && E >= 1 && E <= fast::MAXE && ws_size >= fast::W_END) {
    int S = (E + 255) / 256;
    if (S < 1) S = 1;
    fast::bin_kernel<<<256, fast::ANT, 0, stream>>>((const int*)d_in[1], (const int*)d_in[2],
                                                    (const unsigned short*)d_in[6], (const unsigned short*)d_in[7],
                                                    (char*)d_ws, N, E, S, 1.0f / (float)N);
    fast::agg_kernel<<<256, fast::BNT, 0, stream>>>((const char*)d_in[0], (const char*)d_in[3], (const char*)d_in[4],
                                                    (const u32*)d_in[5], (char*)d_out, (char*)d_ws, N, S,
                                                    32.0f / (float)N);
    return;
  }
  int G = (N + gen::MAXN - 1) / gen::MAXN;
  if (G < 256) G = 256;
  if (G > N) G = N;
  size_t gmax = ws_size / ((size_t)gen::NW * 16384);
  if (gmax > 65536) gmax = 65536;
  if (gmax < 1) return;
  for (int b0 = 0; b0 < G; b0 += (int)gmax) {
    const int g = (G - b0 < (int)gmax) ? G - b0 : (int)gmax;
    gen::mace_kernel<<<g, gen::NT, 0, stream>>>(
        (const char*)d_in[0], (const int*)d_in[1], (const int*)d_in[2], (const char*)d_in[3], (const char*)d_in[4],
        (const unsigned short*)d_in[5], (const char*)d_in[6], (const char*)d_in[7], (char*)d_out, (float*)d_ws, N, E,
        b0, G, (float)gen::NB / (float)N);
  }
}
```
